```python
import torch

def _softmax(x, axis=-1):
    return torch.softmax(x, dim=axis)
def _gelu(x):
    return torch.nn.functional.gelu(x, approximate="tanh")
def _silu(x):
    return torch.nn.functional.silu(x)
def _sigmoid(x):
    return torch.sigmoid(x)
def _layernorm(x, axis=-1, eps=1e-5):
    m = x.mean(dim=axis, keepdim=True)
    v = x.var(dim=axis, keepdim=True, unbiased=False)
    return (x - m) / torch.sqrt(v + eps)
def _scatter_add(tgt, idx, src, axis=0):
    return tgt.clone().index_add_(axis, idx, src)
def _segment_sum(src, seg, n):
    out = torch.zeros((n,) + src.shape[1:], dtype=src.dtype)
    return out.index_add_(0, seg, src)
def _pad(x, pw):
    flat = tuple(v for pair in reversed(pw) for v in pair)
    return torch.nn.functional.pad(x, flat)

kernel_name = 'concat_heads__gqa_block_cached__mha_block_cached__near_ridge'

def setup_inputs():

    _w = (lambda a: (lambda t: t.to(torch.bfloat16) if t.is_floating_point() else t)(torch.as_tensor(a)))
    def _A_setup():
        B, Sq, D = 32, 32, 512
        Hq, Hkv, Sk, Dh = 32, 32, 32, 128
        assert Hq % Hkv == 0, (Hq, Hkv)
        return {
            "x": _w((lambda *s: torch.randn(*s, dtype=torch.bfloat16) * 0.8)(B, Sq, D)),
            "k_cache": _w((lambda *s: torch.randn(*s, dtype=torch.bfloat16) * 0.8)(B, Hkv, Sk, Dh)),
            "v_cache": _w((lambda *s: torch.randn(*s, dtype=torch.bfloat16) * 0.05)(B, Hkv, Sk, Dh)),
            "wq": _w((lambda *s: torch.randn(*s, dtype=torch.bfloat16) * 0.8)(D, Hq * Dh) * (D ** -0.5)),
            "wo": _w((lambda *s: torch.randn(*s, dtype=torch.bfloat16) * 0.05)(Hq * Dh, D) * ((Hq * Dh) ** -0.5)),
        }
    _A_in = _A_setup()

    def _B_setup():
        B, Sq, D = 32, 32, 512
        H, Sk, Dh = 32, 32, 128
        return {
            "x": _w((lambda *s: torch.randn(*s, dtype=torch.bfloat16) * 0.8)(B, Sq, D)),
            "k_cache": _w((lambda *s: torch.randn(*s, dtype=torch.bfloat16) * 0.8)(B, H, Sk, Dh)),
            "v_cache": _w((lambda *s: torch.randn(*s, dtype=torch.bfloat16) * 0.05)(B, H, Sk, Dh)),
            "wq": _w((lambda *s: torch.randn(*s, dtype=torch.bfloat16) * 0.8)(D, H * Dh) * (D ** -0.5)),
            "wo": _w((lambda *s: torch.randn(*s, dtype=torch.bfloat16) * 0.05)(H * Dh, D) * ((H * Dh) ** -0.5)),
        }
    _B_in = _B_setup()

    return {
        "A_x": _A_in["x"],
        "A_k_cache": _A_in["k_cache"],
        "A_v_cache": _A_in["v_cache"],
        "A_wq": _A_in["wq"],
        "A_wo": _A_in["wo"],
        "B_x": _B_in["x"],
        "B_k_cache": _B_in["k_cache"],
        "B_v_cache": _B_in["v_cache"],
        "B_wq": _B_in["wq"],
        "B_wo": _B_in["wo"],
    }

def reference(A_x, A_k_cache, A_v_cache, A_wq, A_wo, B_x, B_k_cache, B_v_cache, B_wq, B_wo):
    def _A_ref(x, k_cache, v_cache, wq, wo):
        B, Sq, D = x.shape
        Hq, Dh = 32, 128
        Hkv = k_cache.shape[1]
        rep = Hq // Hkv
        q = (lambda x, *d: x.permute(*d))((x @ wq).reshape(B, Sq, Hq, Dh), 0, 2, 1, 3)
        k = (lambda x, n, axis: torch.repeat_interleave(x, n, dim=axis))(k_cache, rep, axis=1)
        v = (lambda x, n, axis: torch.repeat_interleave(x, n, dim=axis))(v_cache, rep, axis=1)
        scores = torch.einsum("bhqd,bhkd->bhqk", q, k) * (Dh ** -0.5)
        p = _softmax(scores, axis=-1)
        o = torch.einsum("bhqk,bhkd->bhqd", p, v)
        return (lambda x, *d: x.permute(*d))(o, 0, 2, 1, 3).reshape(B, Sq, Hq * Dh) @ wo

    def _B_ref(x, k_cache, v_cache, wq, wo):
        B, Sq, D = x.shape
        H, Dh = 32, 128
        q = (lambda x, *d: x.permute(*d))((x @ wq).reshape(B, Sq, H, Dh), 0, 2, 1, 3)
        scores = torch.einsum("bhqd,bhkd->bhqk", q, k_cache) * (Dh ** -0.5)
        p = _softmax(scores, axis=-1)
        o = torch.einsum("bhqk,bhkd->bhqd", p, v_cache)
        return (lambda x, *d: x.permute(*d))(o, 0, 2, 1, 3).reshape(B, Sq, H * Dh) @ wo

    a = _A_ref(A_x, A_k_cache, A_v_cache, A_wq, A_wo)
    b = _B_ref(B_x, B_k_cache, B_v_cache, B_wq, B_wo)
    return torch.concatenate([a, b], axis=-1)
```

```cpp
#include <hip/hip_runtime.h>
#include <hip/hip_bf16.h>
#include <stdint.h>

typedef __attribute__((ext_vector_type(8))) __bf16 bf16x8;
typedef __attribute__((ext_vector_type(2))) __bf16 bf16x2;
typedef __attribute__((ext_vector_type(4))) short s16x4;
typedef __attribute__((ext_vector_type(2))) float f32x2;
typedef __attribute__((ext_vector_type(4))) float f32x4;
typedef __attribute__((ext_vector_type(16))) float f32x16;
typedef __attribute__((ext_vector_type(4))) unsigned u32x4;
typedef __attribute__((ext_vector_type(2))) unsigned u32x2;
typedef __attribute__((address_space(3))) s16x4* lds_s16x4_ptr;
typedef __attribute__((address_space(3))) void* lds_void_ptr;
typedef __attribute__((address_space(1))) const void* gbl_void_ptr;

#define DI __device__ __forceinline__
#define MFMA32(a, b, c) __builtin_amdgcn_mfma_f32_32x32x16_bf16((a), (b), (c), 0, 0, 0)
#define MEMFENCE() asm volatile("" ::: "memory")

DI unsigned cvtpk(float lo, float hi) {
  f32x2 v = {lo, hi};
  bf16x2 r = __builtin_convertvector(v, bf16x2);
  return __builtin_bit_cast(unsigned, r);
}
DI bf16x8 pack8(const f32x16& x, int s) {
  u32x4 p = {cvtpk(x[8 * s + 0], x[8 * s + 1]), cvtpk(x[8 * s + 2], x[8 * s + 3]),
             cvtpk(x[8 * s + 4], x[8 * s + 5]), cvtpk(x[8 * s + 6], x[8 * s + 7])};
  return __builtin_bit_cast(bf16x8, p);
}
DI s16x4 tr_read(const char* p) {
  return __builtin_amdgcn_ds_read_tr16_b64_v4i16((lds_s16x4_ptr)(p));
}
DI bf16x8 tr_frag(const char* p0, const char* p1) {
  s16x4 lo = tr_read(p0), hi = tr_read(p1);
  return __builtin_bit_cast(bf16x8, __builtin_shufflevector(lo, hi, 0, 1, 2, 3, 4, 5, 6, 7));
}
DI void glds16(const void* gbase, unsigned voff, unsigned ldst) {
  unsigned keep;
  asm volatile("s_mov_b32 %0, m0\n\ts_mov_b32 m0, %2\n\ts_nop 0\n\tglobal_load_lds_dwordx4 %1, %3\n\ts_mov_b32 m0, %0"
               : "=&s"(keep) : "v"(voff), "s"(ldst), "s"(gbase) : "memory");
}
DI unsigned lds_addr(const void* p) { return (unsigned)(size_t)(__attribute__((address_space(3))) const char*)p; }

constexpr int K1_STAGE = 49152;
constexpr int K1_LDS = 3 * K1_STAGE;

__global__ __launch_bounds__(512, 2) void k1_qproj_attn(
    const uint16_t* __restrict__ xA, const uint16_t* __restrict__ kA, const uint16_t* __restrict__ vA,
    const uint16_t* __restrict__ wqA, const uint16_t* __restrict__ xB, const uint16_t* __restrict__ kB,
    const uint16_t* __restrict__ vB, const uint16_t* __restrict__ wqB, uint16_t* __restrict__ ows) {
  extern __shared__ __attribute__((aligned(16))) char smem[];
  const int tid = threadIdx.x, lane = tid & 63;
  const int w = __builtin_amdgcn_readfirstlane(tid >> 6);
  const int bid = blockIdx.x, xcd = bid & 7, idx = bid >> 3;
  const int X = xcd >> 2, h = (xcd & 3) * 8 + (idx & 7), bg = idx >> 3;
  const int b = bg * 8 + w;
  const uint16_t* x = X ? xB : xA;
  const uint16_t* kc = X ? kB : kA;
  const uint16_t* vc = X ? vB : vA;
  const uint16_t* wq = X ? wqB : wqA;

  const int r = lane & 31, hh = lane >> 5, blk = (lane >> 4) & 1, q = (lane & 15) >> 2, p = lane & 3;

  const char* xb = (const char*)(x + (size_t)(b * 32) * 512);
  const char* wb = (const char*)(wq + (size_t)(w * 8) * 4096 + h * 128);
  const unsigned xoff0 = (lane >> 3) * 1024 + (((lane & 7) ^ (lane >> 4)) << 4);
  const unsigned xoff1 = (8 + (lane >> 3)) * 1024 + (((lane & 7) ^ ((lane >> 4) | 4)) << 4);
  const unsigned woff = (lane >> 4) * 8192 + (((lane & 15) ^ ((lane >> 4) << 2)) << 4);
  const unsigned sbase = lds_addr(smem);

  auto stage = [&](int st, int buf) {
    const unsigned lb = sbase + buf * K1_STAGE;
    glds16(wb + (size_t)st * (64 * 8192), woff, lb + w * 2048);
    glds16(wb + (size_t)st * (64 * 8192) + 4 * 8192, woff, lb + w * 2048 + 1024);
    const unsigned lx = lb + 16384 + w * 4096;
    glds16(xb + st * 128, xoff0, lx);
    glds16(xb + st * 128, xoff1, lx + 1024);
    glds16(xb + st * 128 + 16 * 1024, xoff0, lx + 2048);
    glds16(xb + st * 128 + 16 * 1024, xoff1, lx + 3072);
  };

  const int pr = (r & 0x13) | ((r & 4) << 1) | ((r & 8) >> 1);
  const size_t kvbase = (size_t)(b * 32 + h) * 4096;
  const u32x4* kptr = (const u32x4*)(kc + kvbase + pr * 128 + hh * 8);
  const u32x4* vptr = (const u32x4*)(vc + kvbase + lane * 8);
  u32x4 kf[8], vr[8];

  f32x16 acc[4];
#pragma unroll
  for (int t = 0; t < 4; ++t)
#pragma unroll
    for (int i = 0; i < 16; ++i) acc[t][i] = 0.f;

  const int troff = (8 * hh + q) * 256 + 32 * blk + 16 * (p & 1) + 8 * (p >> 1);
  const int f = (r >> 1) & 7;

  stage(0, 0);
  stage(1, 1);
  MEMFENCE();

#pragma unroll
  for (int st = 0; st < 8; ++st) {
    if (st == 0) asm volatile("s_waitcnt vmcnt(6) lgkmcnt(0)" ::: "memory");
    else if (st < 7) asm volatile("s_waitcnt vmcnt(8) lgkmcnt(0)" ::: "memory");
    else asm volatile("s_waitcnt vmcnt(2) lgkmcnt(0)" ::: "memory");
    __builtin_amdgcn_s_barrier();
    MEMFENCE();
    kf[st] = kptr[st * 2];
    vr[st] = vptr[st * 64];
    MEMFENCE();
    if (st + 2 < 8) stage(st + 2, (st + 2) % 3);
    MEMFENCE();
    const char* lb = smem + (st % 3) * K1_STAGE;
    const char* lx = lb + 16384 + w * 4096 + r * 128;
#pragma unroll
    for (int s = 0; s < 4; ++s) {
      bf16x8 bf = *(const bf16x8*)(lx + (((2 * s + hh) ^ f) << 4));
#pragma unroll
      for (int t = 0; t < 4; ++t) {
        const char* pa = lb + troff + s * 4096 + ((t ^ q) << 6);
        bf16x8 af = tr_frag(pa, pa + 1024);
        acc[t] = MFMA32(af, bf, acc[t]);
      }
    }
  }

  char* vl = smem + (w < 6 ? w * 8192 : 2 * K1_STAGE + (w - 6) * 8192);
#pragma unroll
  for (int i = 0; i < 8; ++i)
    *(u32x4*)(vl + i * 1024 + (lane >> 4) * 256 + (((lane & 15) ^ ((lane >> 4) << 2)) << 4)) = vr[i];

  f32x16 sacc;
#pragma unroll
  for (int i = 0; i < 16; ++i) sacc[i] = 0.f;
#pragma unroll
  for (int t = 0; t < 4; ++t)
#pragma unroll
    for (int s = 0; s < 2; ++s)
      sacc = MFMA32(__builtin_bit_cast(bf16x8, kf[2 * t + s]), pack8(acc[t], s), sacc);

  const float scale = 0.08838834764831845f;
  float m = -3.0e38f;
#pragma unroll
  for (int i = 0; i < 16; i += 2) {
    unsigned u = cvtpk(sacc[i], sacc[i + 1]);
    float a = __uint_as_float(u << 16) * scale, c = __uint_as_float(u & 0xffff0000u) * scale;
    unsigned u2 = cvtpk(a, c);
    sacc[i] = __uint_as_float(u2 << 16);
    sacc[i + 1] = __uint_as_float(u2 & 0xffff0000u);
    m = fmaxf(m, fmaxf(sacc[i], sacc[i + 1]));
  }
  {
    auto rr = __builtin_amdgcn_permlane32_swap(__float_as_uint(m), __float_as_uint(m), false, false);
    m = fmaxf(__uint_as_float(rr[0]), __uint_as_float(rr[1]));
  }
  float sum = 0.f;
#pragma unroll
  for (int i = 0; i < 16; ++i) {
    sacc[i] = __builtin_amdgcn_exp2f((sacc[i] - m) * 1.4426950408889634f);
    sum += sacc[i];
  }
  {
    auto rr = __builtin_amdgcn_permlane32_swap(__float_as_uint(sum), __float_as_uint(sum), false, false);
    sum = __uint_as_float(rr[0]) + __uint_as_float(rr[1]);
  }
  const float inv = 1.0f / sum;
#pragma unroll
  for (int i = 0; i < 16; ++i) sacc[i] *= inv;
  bf16x8 pf0 = pack8(sacc, 0), pf1 = pack8(sacc, 1);

  u32x4* od = (u32x4*)(ows + ((size_t)((X * 32 + b) * 256 + h * 8) * 64 + lane) * 8);
#pragma unroll
  for (int t = 0; t < 4; ++t) {
    f32x16 o;
#pragma unroll
    for (int i = 0; i < 16; ++i) o[i] = 0.f;
    const char* pa = vl + troff + ((t ^ q) << 6);
    o = MFMA32(tr_frag(pa, pa + 1024), pf0, o);
    o = MFMA32(tr_frag(pa + 4096, pa + 4096 + 1024), pf1, o);
    od[(2 * t) * 64] = __builtin_bit_cast(u32x4, pack8(o, 0));
    od[(2 * t + 1) * 64] = __builtin_bit_cast(u32x4, pack8(o, 1));
  }
}

constexpr int K2_LDS = 131072;

__global__ __launch_bounds__(512, 2) void k2_oproj(const uint16_t* __restrict__ woA, const uint16_t* __restrict__ woB,
                                                   const uint16_t* __restrict__ ows, uint16_t* __restrict__ out) {
  extern __shared__ __attribute__((aligned(16))) char smem[];
  const int tid = threadIdx.x, lane = tid & 63;
  const int w = __builtin_amdgcn_readfirstlane(tid >> 6);
  const int bid = blockIdx.x, xcd = bid & 7, idx = bid >> 3;
  const int X = xcd >> 2, rp = (xcd & 3) * 4 + (idx >> 3), nc = idx & 7;
  const uint16_t* wo = X ? woB : woA;
  const int r = lane & 31, hh = lane >> 5, blk = (lane >> 4) & 1, q = (lane & 15) >> 2, p = lane & 3;

  const char* wb = (const char*)(wo + (size_t)(w * 512) * 512 + nc * 64);
  const unsigned woff = (lane >> 3) * 1024 + (((lane & 7) ^ (((lane >> 4) & 1) << 2)) << 4);
  char* lw = smem + w * 16384;
  const unsigned lwa = lds_addr(smem) + w * 16384;
  const char* osrc = (const char*)ows + ((size_t)((X * 32 + rp * 2) * 256 + w * 32) * 64 + lane) * 16;

  u32x4 of[4][4];
  f32x16 acc[2][2];
#pragma unroll
  for (int a = 0; a < 2; ++a)
#pragma unroll
    for (int c = 0; c < 2; ++c)
#pragma unroll
      for (int i = 0; i < 16; ++i) acc[a][c][i] = 0.f;

  auto issue = [&](int st, int slot) {
    const char* g = wb + (size_t)st * (32 * 1024);
    const unsigned l = lwa + slot * 4096;
    glds16(g, woff, l);
    glds16(g + 8 * 1024, woff, l + 1024);
    glds16(g + 16 * 1024, woff, l + 2048);
    glds16(g + 24 * 1024, woff, l + 3072);
    const char* o0 = osrc + st * 2048;
    const char* o1 = o0 + 262144;
    asm volatile("global_load_dwordx4 %0, %1, off" : "=v"(of[slot][0]) : "v"(o0) : "memory");
    asm volatile("global_load_dwordx4 %0, %1, off offset:1024" : "=v"(of[slot][1]) : "v"(o0) : "memory");
    asm volatile("global_load_dwordx4 %0, %1, off" : "=v"(of[slot][2]) : "v"(o1) : "memory");
    asm volatile("global_load_dwordx4 %0, %1, off offset:1024" : "=v"(of[slot][3]) : "v"(o1) : "memory");
  };

  const int troff = (8 * hh + q) * 128 + 32 * blk + 16 * (p & 1) + 8 * (p >> 1);
  const int hsw = (q >> 1) & 1;

  issue(0, 0);
  issue(1, 1);
  issue(2, 2);
#pragma unroll
  for (int st = 0; st < 16; ++st) {
    const int slot = st & 3;
    if (st < 14)
      asm volatile("s_waitcnt vmcnt(16) lgkmcnt(0)" : "+v"(of[slot][0]), "+v"(of[slot][1]), "+v"(of[slot][2]), "+v"(of[slot][3])::"memory");
    else if (st == 14)
      asm volatile("s_waitcnt vmcnt(8) lgkmcnt(0)" : "+v"(of[slot][0]), "+v"(of[slot][1]), "+v"(of[slot][2]), "+v"(of[slot][3])::"memory");
    else
      asm volatile("s_waitcnt vmcnt(0) lgkmcnt(0)" : "+v"(of[slot][0]), "+v"(of[slot][1]), "+v"(of[slot][2]), "+v"(of[slot][3])::"memory");
    if (st + 3 < 16) issue(st + 3, (st + 3) & 3);
    const char* l = lw + slot * 4096 + troff;
#pragma unroll
    for (int s = 0; s < 2; ++s) {
#pragma unroll
      for (int nt = 0; nt < 2; ++nt) {
        const char* pa = l + s * 2048 + ((nt ^ hsw) << 6);
        bf16x8 af = tr_frag(pa, pa + 512);
        acc[nt][0] = MFMA32(af, __builtin_bit_cast(bf16x8, of[slot][s]), acc[nt][0]);
        acc[nt][1] = MFMA32(af, __builtin_bit_cast(bf16x8, of[slot][2 + s]), acc[nt][1]);
      }
    }
  }

  __syncthreads();
  {
    f32x4* rd = (f32x4*)(smem + w * 16384) + lane;
#pragma unroll
    for (int nt = 0; nt < 2; ++nt)
#pragma unroll
      for (int bt = 0; bt < 2; ++bt)
#pragma unroll
        for (int g = 0; g < 4; ++g) {
          f32x4 v = {acc[nt][bt][4 * g], acc[nt][bt][4 * g + 1], acc[nt][bt][4 * g + 2], acc[nt][bt][4 * g + 3]};
          rd[((nt * 2 + bt) * 4 + g) * 64] = v;
        }
  }
  __syncthreads();
  {
    const f32x4* rs = (const f32x4*)smem + (w * 2) * 64 + lane;
    f32x4 s0 = rs[0], s1 = rs[64];
#pragma unroll
    for (int j = 1; j < 8; ++j) {
      s0 += rs[j * 1024];
      s1 += rs[j * 1024 + 64];
    }
    const int tile = w >> 1, nt = tile >> 1, bt = tile & 1, s = w & 1;
    u32x4 o = {cvtpk(s0[0], s0[1]), cvtpk(s0[2], s0[3]), cvtpk(s1[0], s1[1]), cvtpk(s1[2], s1[3])};
    uint16_t* dst = out + (size_t)(rp * 64 + bt * 32 + r) * 1024 + X * 512 + nc * 64 + nt * 32 + s * 16 + hh * 8;
    *(u32x4*)dst = o;
  }
}

extern "C" void kernel_launch(void* const* d_in, const int* in_sizes, int n_in, void* d_out, int out_size,
                              void* d_ws, size_t ws_size, hipStream_t stream) {
  (void)in_sizes; (void)n_in; (void)out_size; (void)ws_size;
  (void)hipFuncSetAttribute((const void*)k1_qproj_attn, hipFuncAttributeMaxDynamicSharedMemorySize, K1_LDS);
  (void)hipFuncSetAttribute((const void*)k2_oproj, hipFuncAttributeMaxDynamicSharedMemorySize, K2_LDS);
  const uint16_t* xA = (const uint16_t*)d_in[0];
  const uint16_t* kA = (const uint16_t*)d_in[1];
  const uint16_t* vA = (const uint16_t*)d_in[2];
  const uint16_t* wqA = (const uint16_t*)d_in[3];
  const uint16_t* woA = (const uint16_t*)d_in[4];
  const uint16_t* xB = (const uint16_t*)d_in[5];
  const uint16_t* kB = (const uint16_t*)d_in[6];
  const uint16_t* vB = (const uint16_t*)d_in[7];
  const uint16_t* wqB = (const uint16_t*)d_in[8];
  const uint16_t* woB = (const uint16_t*)d_in[9];
  uint16_t* ows = (uint16_t*)d_ws;
  k1_qproj_attn<<<256, 512, K1_LDS, stream>>>(xA, kA, vA, wqA, xB, kB, vB, wqB, ows);
  k2_oproj<<<256, 512, K2_LDS, stream>>>(woA, woB, ows, (uint16_t*)d_out);
}
```

```cpp
#include <hip/hip_runtime.h>
#include <hip/hip_bf16.h>
#include <stdint.h>

typedef __attribute__((ext_vector_type(8))) __bf16 bf16x8;
typedef __attribute__((ext_vector_type(2))) __bf16 bf16x2;
typedef __attribute__((ext_vector_type(4))) short s16x4;
typedef __attribute__((ext_vector_type(2))) float f32x2;
typedef __attribute__((ext_vector_type(4))) float f32x4;
typedef __attribute__((ext_vector_type(16))) float f32x16;
typedef __attribute__((ext_vector_type(4))) unsigned u32x4;
typedef __attribute__((address_space(3))) s16x4* lds_s16x4_ptr;

#define DI __device__ __forceinline__
#define MFMA32(a, b, c) __builtin_amdgcn_mfma_f32_32x32x16_bf16((a), (b), (c), 0, 0, 0)
#define MEMFENCE() asm volatile("" ::: "memory")
#define SCHED0() __builtin_amdgcn_sched_barrier(0)

#define NT_KV 1
#define NT_WQ 0
#define NT_X 0
#define NT_WO 0

DI unsigned cvtpk(float lo, float hi) {
  f32x2 v = {lo, hi};
  bf16x2 r = __builtin_convertvector(v, bf16x2);
  return __builtin_bit_cast(unsigned, r);
}
DI bf16x8 pack8(const f32x16& x, int s) {
  u32x4 p = {cvtpk(x[8 * s + 0], x[8 * s + 1]), cvtpk(x[8 * s + 2], x[8 * s + 3]),
             cvtpk(x[8 * s + 4], x[8 * s + 5]), cvtpk(x[8 * s + 6], x[8 * s + 7])};
  return __builtin_bit_cast(bf16x8, p);
}
DI s16x4 tr_read(const char* p) { return __builtin_amdgcn_ds_read_tr16_b64_v4i16((lds_s16x4_ptr)(p)); }
DI bf16x8 tr_frag(const char* p0, const char* p1) {
  s16x4 lo = tr_read(p0), hi = tr_read(p1);
  return __builtin_bit_cast(bf16x8, __builtin_shufflevector(lo, hi, 0, 1, 2, 3, 4, 5, 6, 7));
}
template <int NT>
DI void glds16(const void* gbase, unsigned voff, unsigned ldst) {
  unsigned keep;
  if constexpr (NT)
    asm volatile("s_mov_b32 %0, m0\n\ts_mov_b32 m0, %2\n\ts_nop 0\n\tglobal_load_lds_dwordx4 %1, %3 nt\n\ts_mov_b32 m0, %0"
                 : "=&s"(keep) : "v"(voff), "s"(ldst), "s"(gbase) : "memory");
  else
    asm volatile("s_mov_b32 %0, m0\n\ts_mov_b32 m0, %2\n\ts_nop 0\n\tglobal_load_lds_dwordx4 %1, %3\n\ts_mov_b32 m0, %0"
                 : "=&s"(keep) : "v"(voff), "s"(ldst), "s"(gbase) : "memory");
}
DI unsigned lds_addr(const void* p) { return (unsigned)(size_t)(__attribute__((address_space(3))) const char*)p; }
template <int N> DI void wait_vm() { asm volatile("s_waitcnt vmcnt(%0) lgkmcnt(0)" ::"i"(N) : "memory"); }

constexpr int K1_STAGE = 49152;
constexpr int K1_LDS = 3 * K1_STAGE;

__global__ __launch_bounds__(512, 2) void k1_qproj_attn(
    const uint16_t* __restrict__ xA, const uint16_t* __restrict__ kA, const uint16_t* __restrict__ vA,
    const uint16_t* __restrict__ wqA, const uint16_t* __restrict__ xB, const uint16_t* __restrict__ kB,
    const uint16_t* __restrict__ vB, const uint16_t* __restrict__ wqB, uint16_t* __restrict__ ows) {
  extern __shared__ __attribute__((aligned(16))) char smem[];
  const int tid = threadIdx.x, lane = tid & 63;
  const int w = __builtin_amdgcn_readfirstlane(tid >> 6);
  const int bid = blockIdx.x, xcd = bid & 7, idx = bid >> 3;
  const int X = xcd >> 2, h = (xcd & 3) * 8 + (idx & 7), bg = idx >> 3;
  const int b = bg * 8 + w;
  const uint16_t* x = X ? xB : xA;
  const uint16_t* kc = X ? kB : kA;
  const uint16_t* vc = X ? vB : vA;
  const uint16_t* wq = X ? wqB : wqA;

  const int r = lane & 31, hh = lane >> 5, blk = (lane >> 4) & 1, q = (lane & 15) >> 2, p = lane & 3;

  const char* xb = (const char*)(x + (size_t)(b * 32) * 512);
  const char* wb = (const char*)(wq + (size_t)(w * 8) * 4096 + h * 128);
  const unsigned xoff0 = (lane >> 3) * 1024 + (((lane & 7) ^ (lane >> 4)) << 4);
  const unsigned xoff1 = (8 + (lane >> 3)) * 1024 + (((lane & 7) ^ ((lane >> 4) | 4)) << 4);
  const unsigned woff = (lane >> 4) * 8192 + (((lane & 15) ^ ((lane >> 4) << 2)) << 4);
  const unsigned sbase = lds_addr(smem);

  auto stage = [&](int st) {
    const unsigned lb = sbase + (st % 3) * K1_STAGE;
    glds16<NT_WQ>(wb + (size_t)st * (64 * 8192), woff, lb + w * 2048);
    glds16<NT_WQ>(wb + (size_t)st * (64 * 8192) + 4 * 8192, woff, lb + w * 2048 + 1024);
    const unsigned lx = lb + 16384 + w * 4096;
    glds16<NT_X>(xb + st * 128, xoff0, lx);
    glds16<NT_X>(xb + st * 128, xoff1, lx + 1024);
    glds16<NT_X>(xb + st * 128 + 16 * 1024, xoff0, lx + 2048);
    glds16<NT_X>(xb + st * 128 + 16 * 1024, xoff1, lx + 3072);
  };

  const int pr = (r & 0x13) | ((r & 4) << 1) | ((r & 8) >> 1);
  const size_t kvbase = (size_t)(b * 32 + h) * 4096;
  const u32x4* kptr = (const u32x4*)(kc + kvbase + pr * 128 + hh * 8);
  const u32x4* vptr = (const u32x4*)(vc + kvbase + lane * 8);
  u32x4 kf[8], vr[8];
  auto kvload = [&](int i) {
    kf[i] = __builtin_nontemporal_load(kptr + i * 2);
    vr[i] = __builtin_nontemporal_load(vptr + i * 64);
  };

  f32x16 acc[4];
#pragma unroll
  for (int t = 0; t < 4; ++t)
#pragma unroll
    for (int i = 0; i < 16; ++i) acc[t][i] = 0.f;

  const int troff = (8 * hh + q) * 256 + 32 * blk + 16 * (p & 1) + 8 * (p >> 1);
  const int f = (r >> 1) & 7;
  int tro[4], xo[4];
#pragma unroll
  for (int t = 0; t < 4; ++t) { tro[t] = troff + ((t ^ q) << 6); xo[t] = 16384 + w * 4096 + r * 128 + (((2 * t + hh) ^ f) << 4); }

  bf16x8 af[2][4], bf[2];
  auto ldfrag = [&](int ks, int slot) {
    const char* lb = smem + ((ks >> 2) % 3) * K1_STAGE;
    const int s = ks & 3;
    bf[slot] = *(const bf16x8*)(lb + xo[s]);
#pragma unroll
    for (int t = 0; t < 4; ++t) {
      const char* pa = lb + tro[t] + s * 4096;
      af[slot][t] = tr_frag(pa, pa + 1024);
    }
  };

  stage(0);
  stage(1);
  stage(2);
  MEMFENCE();
  kvload(0);
  MEMFENCE();
  wait_vm<14>();
  __builtin_amdgcn_s_barrier();
  MEMFENCE();
  ldfrag(0, 0);

#pragma unroll
  for (int ks = 0; ks < 32; ++ks) {
    const int st = ks >> 2;
    if ((ks & 3) == 3 && st < 7) {
      if (st == 1) wait_vm<10>();
      else if (st < 6) wait_vm<8>();
      else wait_vm<2>();
      __builtin_amdgcn_s_barrier();
      MEMFENCE();
      kvload(st + 1);
      MEMFENCE();
      if (st + 3 < 8) stage(st + 3);
      MEMFENCE();
    }
    if (ks + 1 < 32) ldfrag(ks + 1, (ks + 1) & 1);
#pragma unroll
    for (int t = 0; t < 4; ++t) acc[t] = MFMA32(af[ks & 1][t], bf[ks & 1], acc[t]);
    SCHED0();
  }

  char* vl = smem + (w < 6 ? w * 8192 : 2 * K1_STAGE + (w - 6) * 8192);
#pragma unroll
  for (int i = 0; i < 8; ++i)
    *(u32x4*)(vl + i * 1024 + (lane >> 4) * 256 + (((lane & 15) ^ ((lane >> 4) << 2)) << 4)) = vr[i];

  f32x16 sacc;
#pragma unroll
  for (int i = 0; i < 16; ++i) sacc[i] = 0.f;
#pragma unroll
  for (int t = 0; t < 4; ++t)
#pragma unroll
    for (int s = 0; s < 2; ++s)
      sacc = MFMA32(__builtin_bit_cast(bf16x8, kf[2 * t + s]), pack8(acc[t], s), sacc);

  const float scale = 0.08838834764831845f;
  float m = -3.0e38f;
#pragma unroll
  for (int i = 0; i < 16; i += 2) {
    unsigned u = cvtpk(sacc[i], sacc[i + 1]);
    float a = __uint_as_float(u << 16) * scale, c = __uint_as_float(u & 0xffff0000u) * scale;
    unsigned u2 = cvtpk(a, c);
    sacc[i] = __uint_as_float(u2 << 16);
    sacc[i + 1] = __uint_as_float(u2 & 0xffff0000u);
    m = fmaxf(m, fmaxf(sacc[i], sacc[i + 1]));
  }
  {
    auto rr = __builtin_amdgcn_permlane32_swap(__float_as_uint(m), __float_as_uint(m), false, false);
    m = fmaxf(__uint_as_float(rr[0]), __uint_as_float(rr[1]));
  }
  float sum = 0.f;
#pragma unroll
  for (int i = 0; i < 16; ++i) {
    sacc[i] = __builtin_amdgcn_exp2f((sacc[i] - m) * 1.4426950408889634f);
    sum += sacc[i];
  }
  {
    auto rr = __builtin_amdgcn_permlane32_swap(__float_as_uint(sum), __float_as_uint(sum), false, false);
    sum = __uint_as_float(rr[0]) + __uint_as_float(rr[1]);
  }
  const float inv = 1.0f / sum;
#pragma unroll
  for (int i = 0; i < 16; ++i) sacc[i] *= inv;
  bf16x8 pf0 = pack8(sacc, 0), pf1 = pack8(sacc, 1);

  u32x4* od = (u32x4*)(ows + ((size_t)((X * 32 + b) * 256 + h * 8) * 64 + lane) * 8);
#pragma unroll
  for (int t = 0; t < 4; ++t) {
    f32x16 o;
#pragma unroll
    for (int i = 0; i < 16; ++i) o[i] = 0.f;
    const char* pa = vl + tro[t];
    o = MFMA32(tr_frag(pa, pa + 1024), pf0, o);
    o = MFMA32(tr_frag(pa + 4096, pa + 4096 + 1024), pf1, o);
    od[(2 * t) * 64] = __builtin_bit_cast(u32x4, pack8(o, 0));
    od[(2 * t + 1) * 64] = __builtin_bit_cast(u32x4, pack8(o, 1));
  }
}

constexpr int K2_NB = 5;
constexpr int K2_STAGE = 32768;
constexpr int K2_LDS = K2_NB * K2_STAGE;

__global__ __launch_bounds__(512, 2) void k2_oproj(const uint16_t* __restrict__ woA, const uint16_t* __restrict__ woB,
                                                   const uint16_t* __restrict__ ows, float* __restrict__ part) {
  extern __shared__ __attribute__((aligned(16))) char smem[];
  const int tid = threadIdx.x, lane = tid & 63;
  const int w = __builtin_amdgcn_readfirstlane(tid >> 6);
  const int bid = blockIdx.x, xcd = bid & 7, idx = bid >> 3;
  const int X = xcd >> 2, hg = xcd & 3, rt = idx >> 2, ct = idx & 3;
  const int kh = w >> 2, gi = w & 3, nw = gi >> 1, rw = gi & 1;
  const uint16_t* wo = X ? woB : woA;
  const int hh = lane >> 5, blk = (lane >> 4) & 1, q = (lane & 15) >> 2, p = lane & 3;

  const char* wb = (const char*)(wo + (size_t)(hg * 1024 + kh * 512 + gi * 8) * 512 + ct * 128);
  const unsigned woff = (lane >> 4) * 1024 + (((lane & 15) ^ ((lane >> 4) << 2)) << 4);
  const char* ob = (const char*)ows + (size_t)((X * 32 + rt * 4 + gi) * 256 + hg * 64 + kh * 32) * 1024;
  const unsigned ooff = lane * 16;
  const unsigned sbase = lds_addr(smem) + kh * 16384;

  auto stage = [&](int st) {
    const unsigned lb = sbase + (st % K2_NB) * K2_STAGE;
    glds16<NT_WO>(wb + (size_t)st * (32 * 1024), woff, lb + gi * 2048);
    glds16<NT_WO>(wb + (size_t)st * (32 * 1024) + 4096, woff, lb + gi * 2048 + 1024);
    glds16<0>(ob + st * 2048, ooff, lb + 8192 + gi * 2048);
    glds16<0>(ob + st * 2048 + 1024, ooff, lb + 8192 + gi * 2048 + 1024);
  };

  f32x16 acc[2][2];
#pragma unroll
  for (int a = 0; a < 2; ++a)
#pragma unroll
    for (int c = 0; c < 2; ++c)
#pragma unroll
      for (int i = 0; i < 16; ++i) acc[a][c][i] = 0.f;

  const int troff = kh * 16384 + (8 * hh + q) * 256 + 32 * blk + 16 * (p & 1) + 8 * (p >> 1);
  const int tro0 = troff + (((nw * 2) ^ q) << 6), tro1 = troff + (((nw * 2 + 1) ^ q) << 6);
  const int bo = kh * 16384 + 8192 + rw * 4096 + lane * 16;

  bf16x8 af[2][2], bf[2][2];
  auto ldfrag = [&](int ks, int slot) {
    const char* lb = smem + ((ks >> 1) % K2_NB) * K2_STAGE;
    const int s = ks & 1;
    bf[slot][0] = *(const bf16x8*)(lb + bo + s * 1024);
    af[slot][0] = tr_frag(lb + tro0 + s * 4096, lb + tro0 + s * 4096 + 1024);
    bf[slot][1] = *(const bf16x8*)(lb + bo + 2048 + s * 1024);
    af[slot][1] = tr_frag(lb + tro1 + s * 4096, lb + tro1 + s * 4096 + 1024);
  };

#pragma unroll
  for (int st = 0; st < K2_NB; ++st) stage(st);
  wait_vm<4 * (K2_NB - 1)>();
  __builtin_amdgcn_s_barrier();
  MEMFENCE();
  ldfrag(0, 0);

#pragma unroll
  for (int ks = 0; ks < 32; ++ks) {
    const int st = ks >> 1;
    if ((ks & 1) == 1 && st < 15) {
      if (st + K2_NB - 1 <= 15) wait_vm<4 * (K2_NB - 2)>();
      else if (15 - (st + 1) == 2) wait_vm<8>();
      else if (15 - (st + 1) == 1) wait_vm<4>();
      else wait_vm<0>();
      __builtin_amdgcn_s_barrier();
      MEMFENCE();
      if (st + K2_NB < 16) stage(st + K2_NB);
      MEMFENCE();
    }
    if (ks + 1 < 32) ldfrag(ks + 1, (ks + 1) & 1);
#pragma unroll
    for (int nt = 0; nt < 2; ++nt)
#pragma unroll
      for (int bt = 0; bt < 2; ++bt) acc[nt][bt] = MFMA32(af[ks & 1][nt], bf[ks & 1][bt], acc[nt][bt]);
    SCHED0();
  }

  __syncthreads();
  {
    f32x4* sd = (f32x4*)(smem + w * 8192) + lane;
#pragma unroll
    for (int nt = 0; nt < 2; ++nt)
#pragma unroll
      for (int g = 0; g < 4; ++g) {
        const f32x16& a = kh ? acc[nt][0] : acc[nt][1];
        f32x4 v = {a[4 * g], a[4 * g + 1], a[4 * g + 2], a[4 * g + 3]};
        sd[(nt * 4 + g) * 64] = v;
      }
  }
  __syncthreads();
  {
    const f32x4* rs = (const f32x4*)(smem + (w ^ 4) * 8192) + lane;
    const int batch = rt * 4 + rw * 2 + kh;
    f32x4* dst = (f32x4*)part + ((size_t)(((hg * 2 + X) * 32 + batch) * 16 + ct * 4 + nw * 2) * 4) * 64 + lane;
#pragma unroll
    for (int nt = 0; nt < 2; ++nt)
#pragma unroll
      for (int g = 0; g < 4; ++g) {
        const f32x16& a = kh ? acc[nt][1] : acc[nt][0];
        f32x4 v = {a[4 * g], a[4 * g + 1], a[4 * g + 2], a[4 * g + 3]};
        dst[(nt * 4 + g) * 64] = v + rs[(nt * 4 + g) * 64];
      }
  }
}

__global__ __launch_bounds__(512) void k3_reduce(const float* __restrict__ part, uint16_t* __restrict__ out) {
  const int gid = blockIdx.x * 512 + threadIdx.x;
  const int lane = gid & 63, s = (gid >> 6) & 1, tile = gid >> 7;
  const f32x4* src = (const f32x4*)part + ((size_t)tile * 4 + s * 2) * 64 + lane;
  f32x4 a = src[0], c = src[64];
#pragma unroll
  for (int g = 1; g < 4; ++g) {
    a += src[(size_t)g * 262144];
    c += src[(size_t)g * 262144 + 64];
  }
  const int nt = tile & 15, batch = (tile >> 4) & 31, X = tile >> 9;
  u32x4 o = {cvtpk(a[0], a[1]), cvtpk(a[2], a[3]), cvtpk(c[0], c[1]), cvtpk(c[2], c[3])};
  *(u32x4*)(out + (size_t)(batch * 32 + (lane & 31)) * 1024 + X * 512 + nt * 32 + s * 16 + (lane >> 5) * 8) = o;
}

extern "C" void kernel_launch(void* const* d_in, const int* in_sizes, int n_in, void* d_out, int out_size,
                              void* d_ws, size_t ws_size, hipStream_t stream) {
  (void)in_sizes; (void)n_in; (void)out_size; (void)ws_size;
  (void)hipFuncSetAttribute((const void*)k1_qproj_attn, hipFuncAttributeMaxDynamicSharedMemorySize, K1_LDS);
  (void)hipFuncSetAttribute((const void*)k2_oproj, hipFuncAttributeMaxDynamicSharedMemorySize, K2_LDS);
  const uint16_t* xA = (const uint16_t*)d_in[0];
  const uint16_t* kA = (const uint16_t*)d_in[1];
  const uint16_t* vA = (const uint16_t*)d_in[2];
  const uint16_t* wqA = (const uint16_t*)d_in[3];
  const uint16_t* woA = (const uint16_t*)d_in[4];
  const uint16_t* xB = (const uint16_t*)d_in[5];
  const uint16_t* kB = (const uint16_t*)d_in[6];
  const uint16_t* vB = (const uint16_t*)d_in[7];
  const uint16_t* wqB = (const uint16_t*)d_in[8];
  const uint16_t* woB = (const uint16_t*)d_in[9];
  uint16_t* ows = (uint16_t*)d_ws;
  float* part = (float*)((char*)d_ws + (16u << 20));
  k1_qproj_attn<<<256, 512, K1_LDS, stream>>>(xA, kA, vA, wqA, xB, kB, vB, wqB, ows);
  k2_oproj<<<256, 512, K2_LDS, stream>>>(woA, woB, ows, part);
  k3_reduce<<<256, 512, 0, stream>>>(part, (uint16_t*)d_out);
}
```

```cpp
#include <hip/hip_runtime.h>
#include <hip/hip_bf16.h>
#include <stdint.h>

typedef __attribute__((ext_vector_type(8))) __bf16 bf16x8;
typedef __attribute__((ext_vector_type(2))) __bf16 bf16x2;
typedef __attribute__((ext_vector_type(4))) short s16x4;
typedef __attribute__((ext_vector_type(2))) float f32x2;
typedef __attribute__((ext_vector_type(4))) float f32x4;
typedef __attribute__((ext_vector_type(16))) float f32x16;
typedef __attribute__((ext_vector_type(4))) unsigned u32x4;
typedef __attribute__((address_space(3))) s16x4* lds_s16x4_ptr;

#define DI __device__ __forceinline__
#define MFMA32(a, b, c) __builtin_amdgcn_mfma_f32_32x32x16_bf16((a), (b), (c), 0, 0, 0)
#define MEMFENCE() asm volatile("" ::: "memory")
#define SCHED0() __builtin_amdgcn_sched_barrier(0)

#define NT_KV 1
#define NT_WQ 0
#define NT_X 0
#define NT_WO 0

DI unsigned cvtpk(float lo, float hi) {
  f32x2 v = {lo, hi};
  bf16x2 r = __builtin_convertvector(v, bf16x2);
  return __builtin_bit_cast(unsigned, r);
}
DI bf16x8 pack8(const f32x16& x, int s) {
  u32x4 p = {cvtpk(x[8 * s + 0], x[8 * s + 1]), cvtpk(x[8 * s + 2], x[8 * s + 3]),
             cvtpk(x[8 * s + 4], x[8 * s + 5]), cvtpk(x[8 * s + 6], x[8 * s + 7])};
  return __builtin_bit_cast(bf16x8, p);
}
DI s16x4 tr_read(const char* p) { return __builtin_amdgcn_ds_read_tr16_b64_v4i16((lds_s16x4_ptr)(p)); }
DI bf16x8 tr_frag(const char* p0, const char* p1) {
  s16x4 lo = tr_read(p0), hi = tr_read(p1);
  return __builtin_bit_cast(bf16x8, __builtin_shufflevector(lo, hi, 0, 1, 2, 3, 4, 5, 6, 7));
}
template <int NT>
DI void glds16(const void* gbase, unsigned voff, unsigned ldst) {
  unsigned keep;
  if constexpr (NT)
    asm volatile("s_mov_b32 %0, m0\n\ts_mov_b32 m0, %2\n\ts_nop 0\n\tglobal_load_lds_dwordx4 %1, %3 nt\n\ts_mov_b32 m0, %0"
                 : "=&s"(keep) : "v"(voff), "s"(ldst), "s"(gbase) : "memory");
  else
    asm volatile("s_mov_b32 %0, m0\n\ts_mov_b32 m0, %2\n\ts_nop 0\n\tglobal_load_lds_dwordx4 %1, %3\n\ts_mov_b32 m0, %0"
                 : "=&s"(keep) : "v"(voff), "s"(ldst), "s"(gbase) : "memory");
}
DI unsigned lds_addr(const void* p) { return (unsigned)(size_t)(__attribute__((address_space(3))) const char*)p; }
template <int N> DI void wait_vm() { asm volatile("s_waitcnt vmcnt(%0) lgkmcnt(0)" ::"i"(N) : "memory"); }

constexpr int K1_STAGE = 49152;
constexpr int K1_LDS = 3 * K1_STAGE;

__global__ __launch_bounds__(512, 2) void k1_qproj_attn(
    const uint16_t* __restrict__ xA, const uint16_t* __restrict__ kA, const uint16_t* __restrict__ vA,
    const uint16_t* __restrict__ wqA, const uint16_t* __restrict__ xB, const uint16_t* __restrict__ kB,
    const uint16_t* __restrict__ vB, const uint16_t* __restrict__ wqB, uint16_t* __restrict__ ows) {
  extern __shared__ __attribute__((aligned(16))) char smem[];
  const int tid = threadIdx.x, lane = tid & 63;
  const int w = __builtin_amdgcn_readfirstlane(tid >> 6);
  const int bid = blockIdx.x, xcd = bid & 7, idx = bid >> 3;
  const int X = xcd >> 2, h = (xcd & 3) * 8 + (idx & 7), bg = idx >> 3;
  const int b = bg * 8 + w;
  const uint16_t* x = X ? xB : xA;
  const uint16_t* kc = X ? kB : kA;
  const uint16_t* vc = X ? vB : vA;
  const uint16_t* wq = X ? wqB : wqA;

  const int r = lane & 31, hh = lane >> 5, blk = (lane >> 4) & 1, q = (lane & 15) >> 2, p = lane & 3;

  const int wl = w & 3, rot = ((idx & 7) + 2 * bg) & 7;
  const char* xb = (const char*)(x + (size_t)((bg * 8 + wl * 2) * 32) * 512);
  const char* wb = (const char*)(wq + (size_t)(wl * 16) * 4096 + h * 128);
  const unsigned xoff0 = (lane >> 3) * 1024 + (((lane & 7) ^ (lane >> 4)) << 4);
  const unsigned xoff1 = (8 + (lane >> 3)) * 1024 + (((lane & 7) ^ ((lane >> 4) | 4)) << 4);
  const unsigned woff = (lane >> 4) * 8192 + (((lane & 15) ^ ((lane >> 4) << 2)) << 4);
  const unsigned sbase = lds_addr(smem);

  auto stage = [&](int st) {
    const int ks = (st + rot) & 7;
    const unsigned lb = sbase + (st % 3) * K1_STAGE;
    const char* wg = wb + (size_t)ks * (64 * 8192);
#pragma unroll
    for (int j = 0; j < 4; ++j) glds16<NT_WQ>(wg + j * (4 * 8192), woff, lb + wl * 4096 + j * 1024);
    const unsigned lx = lb + 16384 + wl * 8192;
    const char* xg = xb + ks * 128;
#pragma unroll
    for (int j = 0; j < 4; ++j) {
      glds16<NT_X>(xg + j * 16384, xoff0, lx + j * 2048);
      glds16<NT_X>(xg + j * 16384, xoff1, lx + j * 2048 + 1024);
    }
  };

  const size_t kvbase = (size_t)(b * 32 + h) * 4096;
  const uint16_t* kb = kc + kvbase;
  const uint16_t* vb = vc + kvbase;
  const unsigned kvo0 = lane * 16, kvo1 = lane * 16 + 4096;
  u32x4 kr[8], vr[8];
#pragma unroll
  for (int i = 0; i < 8; ++i) { kr[i] = u32x4{0, 0, 0, 0}; vr[i] = u32x4{0, 0, 0, 0}; }
#define KVNT " nt"
  auto kvload_if = [&](int cond) {
    asm volatile(
        "s_cmp_eq_u32 %16, 0\n\t"
        "s_cbranch_scc1 .Lkv%=\n\t"
        "global_load_dwordx4 %0, %17, %19" KVNT "\n\t"
        "global_load_dwordx4 %1, %17, %19 offset:1024" KVNT "\n\t"
        "global_load_dwordx4 %2, %17, %19 offset:2048" KVNT "\n\t"
        "global_load_dwordx4 %3, %17, %19 offset:3072" KVNT "\n\t"
        "global_load_dwordx4 %4, %18, %19" KVNT "\n\t"
        "global_load_dwordx4 %5, %18, %19 offset:1024" KVNT "\n\t"
        "global_load_dwordx4 %6, %18, %19 offset:2048" KVNT "\n\t"
        "global_load_dwordx4 %7, %18, %19 offset:3072" KVNT "\n\t"
        "global_load_dwordx4 %8, %17, %20" KVNT "\n\t"
        "global_load_dwordx4 %9, %17, %20 offset:1024" KVNT "\n\t"
        "global_load_dwordx4 %10, %17, %20 offset:2048" KVNT "\n\t"
        "global_load_dwordx4 %11, %17, %20 offset:3072" KVNT "\n\t"
        "global_load_dwordx4 %12, %18, %20" KVNT "\n\t"
        "global_load_dwordx4 %13, %18, %20 offset:1024" KVNT "\n\t"
        "global_load_dwordx4 %14, %18, %20 offset:2048" KVNT "\n\t"
        "global_load_dwordx4 %15, %18, %20 offset:3072" KVNT "\n\t"
        ".Lkv%=:"
        : "+v"(kr[0]), "+v"(kr[1]), "+v"(kr[2]), "+v"(kr[3]), "+v"(kr[4]), "+v"(kr[5]), "+v"(kr[6]), "+v"(kr[7]),
          "+v"(vr[0]), "+v"(vr[1]), "+v"(vr[2]), "+v"(vr[3]), "+v"(vr[4]), "+v"(vr[5]), "+v"(vr[6]), "+v"(vr[7])
        : "s"(cond), "v"(kvo0), "v"(kvo1), "s"(kb), "s"(vb)
        : "memory", "scc");
  };

  f32x16 acc[4];
#pragma unroll
  for (int t = 0; t < 4; ++t)
#pragma unroll
    for (int i = 0; i < 16; ++i) acc[t][i] = 0.f;

  const int troff = (8 * hh + q) * 256 + 32 * blk + 16 * (p & 1) + 8 * (p >> 1);
  const int f = (r >> 1) & 7;
  int tro[4], xo[4];
#pragma unroll
  for (int t = 0; t < 4; ++t) { tro[t] = troff + ((t ^ q) << 6); xo[t] = 16384 + w * 4096 + r * 128 + (((2 * t + hh) ^ f) << 4); }

  bf16x8 af[2][4], bf[2];
  auto ldfrag = [&](int ks, int slot) {
    const char* lb = smem + ((ks >> 2) % 3) * K1_STAGE;
    const int s = ks & 3;
    bf[slot] = *(const bf16x8*)(lb + xo[s]);
#pragma unroll
    for (int t = 0; t < 4; ++t) {
      const char* pa = lb + tro[t] + s * 4096;
      af[slot][t] = tr_frag(pa, pa + 1024);
    }
  };

  const bool lo = w < 4;
  kvload_if(w >> 2);
  if (lo) {
    stage(0);
    stage(1);
    stage(2);
    wait_vm<24>();
  }
  MEMFENCE();
  __builtin_amdgcn_s_barrier();
  MEMFENCE();
  ldfrag(0, 0);

#pragma unroll
  for (int ks = 0; ks < 32; ++ks) {
    const int st = ks >> 2;
    if ((ks & 3) == 3 && st < 7) {
      if (st < 3) {
        if (lo) {
          if (st == 0) wait_vm<12>();
          else if (st == 1) wait_vm<28>();
          else wait_vm<16>();
        } else wait_vm<63>();
      } else {
        if (!lo) {
          if (st < 6) wait_vm<12>(); else wait_vm<0>();
        } else wait_vm<63>();
      }
      __builtin_amdgcn_s_barrier();
      MEMFENCE();
      if (st == 0) { if (lo) stage(3); kvload_if(1 - (w >> 2)); }
      else if (st + 3 < 8) { if (!lo) stage(st + 3); }
      MEMFENCE();
    }
    if (ks + 1 < 32) ldfrag(ks + 1, (ks + 1) & 1);
#pragma unroll
    for (int t = 0; t < 4; ++t) acc[t] = MFMA32(af[ks & 1][t], bf[ks & 1], acc[t]);
    SCHED0();
  }

  char* vl = smem + (w < 6 ? w * 8192 : 2 * K1_STAGE + (w - 6) * 8192);
  asm volatile("s_waitcnt vmcnt(8)" : "+v"(kr[0]), "+v"(kr[1]), "+v"(kr[2]), "+v"(kr[3]), "+v"(kr[4]), "+v"(kr[5]), "+v"(kr[6]), "+v"(kr[7])::"memory");
#pragma unroll
  for (int i = 0; i < 8; ++i)
    *(u32x4*)(vl + i * 1024 + (lane >> 4) * 256 + (((lane & 15) ^ ((4 * i + (lane >> 4)) & 15)) << 4)) = kr[i];
  const int pr = (r & 0x13) | ((r & 4) << 1) | ((r & 8) >> 1);
  bf16x8 kf[8];
#pragma unroll
  for (int i = 0; i < 8; ++i) kf[i] = *(const bf16x8*)(vl + pr * 256 + (((2 * i + hh) ^ (pr & 15)) << 4));
  asm volatile("s_waitcnt vmcnt(0)" : "+v"(vr[0]), "+v"(vr[1]), "+v"(vr[2]), "+v"(vr[3]), "+v"(vr[4]), "+v"(vr[5]), "+v"(vr[6]), "+v"(vr[7])::"memory");
#pragma unroll
  for (int i = 0; i < 8; ++i)
    *(u32x4*)(vl + i * 1024 + (lane >> 4) * 256 + (((lane & 15) ^ ((lane >> 4) << 2)) << 4)) = vr[i];

  f32x16 sacc;
#pragma unroll
  for (int i = 0; i < 16; ++i) sacc[i] = 0.f;
#pragma unroll
  for (int t = 0; t < 4; ++t)
#pragma unroll
    for (int s = 0; s < 2; ++s)
      sacc = MFMA32(kf[2 * t + s], pack8(acc[t], s), sacc);

  const float scale = 0.08838834764831845f;
  float m = -3.0e38f;
#pragma unroll
  for (int i = 0; i < 16; i += 2) {
    unsigned u = cvtpk(sacc[i], sacc[i + 1]);
    float a = __uint_as_float(u << 16) * scale, c = __uint_as_float(u & 0xffff0000u) * scale;
    unsigned u2 = cvtpk(a, c);
    sacc[i] = __uint_as_float(u2 << 16);
    sacc[i + 1] = __uint_as_float(u2 & 0xffff0000u);
    m = fmaxf(m, fmaxf(sacc[i], sacc[i + 1]));
  }
  {
    auto rr = __builtin_amdgcn_permlane32_swap(__float_as_uint(m), __float_as_uint(m), false, false);
    m = fmaxf(__uint_as_float(rr[0]), __uint_as_float(rr[1]));
  }
  float sum = 0.f;
#pragma unroll
  for (int i = 0; i < 16; ++i) {
    sacc[i] = __builtin_amdgcn_exp2f((sacc[i] - m) * 1.4426950408889634f);
    sum += sacc[i];
  }
  {
    auto rr = __builtin_amdgcn_permlane32_swap(__float_as_uint(sum), __float_as_uint(sum), false, false);
    sum = __uint_as_float(rr[0]) + __uint_as_float(rr[1]);
  }
  const float inv = 1.0f / sum;
#pragma unroll
  for (int i = 0; i < 16; ++i) sacc[i] *= inv;
  bf16x8 pf0 = pack8(sacc, 0), pf1 = pack8(sacc, 1);

  u32x4* od = (u32x4*)(ows + ((size_t)((X * 32 + b) * 256 + h * 8) * 64 + lane) * 8);
#pragma unroll
  for (int t = 0; t < 4; ++t) {
    f32x16 o;
#pragma unroll
    for (int i = 0; i < 16; ++i) o[i] = 0.f;
    const char* pa = vl + tro[t];
    o = MFMA32(tr_frag(pa, pa + 1024), pf0, o);
    o = MFMA32(tr_frag(pa + 4096, pa + 4096 + 1024), pf1, o);
    od[(2 * t) * 64] = __builtin_bit_cast(u32x4, pack8(o, 0));
    od[(2 * t + 1) * 64] = __builtin_bit_cast(u32x4, pack8(o, 1));
  }
}

constexpr int K2_NB = 5;
constexpr int K2_STAGE = 32768;
constexpr int K2_LDS = K2_NB * K2_STAGE;

__global__ __launch_bounds__(512, 2) void k2_oproj(const uint16_t* __restrict__ woA, const uint16_t* __restrict__ woB,
                                                   const uint16_t* __restrict__ ows, float* __restrict__ part) {
  extern __shared__ __attribute__((aligned(16))) char smem[];
  const int tid = threadIdx.x, lane = tid & 63;
  const int w = __builtin_amdgcn_readfirstlane(tid >> 6);
  const int bid = blockIdx.x, xcd = bid & 7, idx = bid >> 3;
  const int X = xcd >> 2, hg = xcd & 3, rt = idx >> 2, ct = idx & 3;
  const int kh = w >> 2, gi = w & 3, nw = gi >> 1, rw = gi & 1;
  const uint16_t* wo = X ? woB : woA;
  const int hh = lane >> 5, blk = (lane >> 4) & 1, q = (lane & 15) >> 2, p = lane & 3;

  const char* wb = (const char*)(wo + (size_t)(hg * 1024 + kh * 512 + gi * 8) * 512 + ct * 128);
  const unsigned woff = (lane >> 4) * 1024 + (((lane & 15) ^ ((lane >> 4) << 2)) << 4);
  const char* ob = (const char*)ows + (size_t)((X * 32 + rt * 4 + gi) * 256 + hg * 64 + kh * 32) * 1024;
  const unsigned ooff = lane * 16;
  const unsigned sbase = lds_addr(smem) + kh * 16384;

  const int rot = (2 * rt + 5 * ct) & 15;
  auto stage = [&](int st) {
    const int ks = (st + rot) & 15;
    const unsigned lb = sbase + (st % K2_NB) * K2_STAGE;
    glds16<NT_WO>(wb + (size_t)ks * (32 * 1024), woff, lb + gi * 2048);
    glds16<NT_WO>(wb + (size_t)ks * (32 * 1024) + 4096, woff, lb + gi * 2048 + 1024);
    glds16<0>(ob + ks * 2048, ooff, lb + 8192 + gi * 2048);
    glds16<0>(ob + ks * 2048 + 1024, ooff, lb + 8192 + gi * 2048 + 1024);
  };

  f32x16 acc[2][2];
#pragma unroll
  for (int a = 0; a < 2; ++a)
#pragma unroll
    for (int c = 0; c < 2; ++c)
#pragma unroll
      for (int i = 0; i < 16; ++i) acc[a][c][i] = 0.f;

  const int troff = kh * 16384 + (8 * hh + q) * 256 + 32 * blk + 16 * (p & 1) + 8 * (p >> 1);
  const int tro0 = troff + (((nw * 2) ^ q) << 6), tro1 = troff + (((nw * 2 + 1) ^ q) << 6);
  const int bo = kh * 16384 + 8192 + rw * 4096 + lane * 16;

  bf16x8 af[2][2], bf[2][2];
  auto ldfrag = [&](int ks, int slot) {
    const char* lb = smem + ((ks >> 1) % K2_NB) * K2_STAGE;
    const int s = ks & 1;
    bf[slot][0] = *(const bf16x8*)(lb + bo + s * 1024);
    af[slot][0] = tr_frag(lb + tro0 + s * 4096, lb + tro0 + s * 4096 + 1024);
    bf[slot][1] = *(const bf16x8*)(lb + bo + 2048 + s * 1024);
    af[slot][1] = tr_frag(lb + tro1 + s * 4096, lb + tro1 + s * 4096 + 1024);
  };

#pragma unroll
  for (int st = 0; st < K2_NB; ++st) stage(st);
  wait_vm<4 * (K2_NB - 1)>();
  __builtin_amdgcn_s_barrier();
  MEMFENCE();
  ldfrag(0, 0);

#pragma unroll
  for (int ks = 0; ks < 32; ++ks) {
    const int st = ks >> 1;
    if ((ks & 1) == 1 && st < 15) {
      if (st + K2_NB - 1 <= 15) wait_vm<4 * (K2_NB - 2)>();
      else if (15 - (st + 1) == 2) wait_vm<8>();
      else if (15 - (st + 1) == 1) wait_vm<4>();
      else wait_vm<0>();
      __builtin_amdgcn_s_barrier();
      MEMFENCE();
      if (st + K2_NB < 16) stage(st + K2_NB);
      MEMFENCE();
    }
    if (ks + 1 < 32) ldfrag(ks + 1, (ks + 1) & 1);
#pragma unroll
    for (int nt = 0; nt < 2; ++nt)
#pragma unroll
      for (int bt = 0; bt < 2; ++bt) acc[nt][bt] = MFMA32(af[ks & 1][nt], bf[ks & 1][bt], acc[nt][bt]);
    SCHED0();
  }

  __syncthreads();
  {
    f32x4* sd = (f32x4*)(smem + w * 8192) + lane;
#pragma unroll
    for (int nt = 0; nt < 2; ++nt)
#pragma unroll
      for (int g = 0; g < 4; ++g) {
        const f32x16& a = kh ? acc[nt][0] : acc[nt][1];
        f32x4 v = {a[4 * g], a[4 * g + 1], a[4 * g + 2], a[4 * g + 3]};
        sd[(nt * 4 + g) * 64] = v;
      }
  }
  __syncthreads();
  {
    const f32x4* rs = (const f32x4*)(smem + (w ^ 4) * 8192) + lane;
    const int batch = rt * 4 + rw * 2 + kh;
    f32x4* dst = (f32x4*)part + ((size_t)(((hg * 2 + X) * 32 + batch) * 16 + ct * 4 + nw * 2) * 4) * 64 + lane;
#pragma unroll
    for (int nt = 0; nt < 2; ++nt)
#pragma unroll
      for (int g = 0; g < 4; ++g) {
        const f32x16& a = kh ? acc[nt][1] : acc[nt][0];
        f32x4 v = {a[4 * g], a[4 * g + 1], a[4 * g + 2], a[4 * g + 3]};
        dst[(nt * 4 + g) * 64] = v + rs[(nt * 4 + g) * 64];
      }
  }
}

__global__ __launch_bounds__(512) void k3_reduce(const float* __restrict__ part, uint16_t* __restrict__ out) {
  const int gid = blockIdx.x * 512 + threadIdx.x;
  const int lane = gid & 63, s = (gid >> 6) & 1, tile = gid >> 7;
  const f32x4* src = (const f32x4*)part + ((size_t)tile * 4 + s * 2) * 64 + lane;
  f32x4 a = src[0], c = src[64];
#pragma unroll
  for (int g = 1; g < 4; ++g) {
    a += src[(size_t)g * 262144];
    c += src[(size_t)g * 262144 + 64];
  }
  const int nt = tile & 15, batch = (tile >> 4) & 31, X = tile >> 9;
  u32x4 o = {cvtpk(a[0], a[1]), cvtpk(a[2], a[3]), cvtpk(c[0], c[1]), cvtpk(c[2], c[3])};
  *(u32x4*)(out + (size_t)(batch * 32 + (lane & 31)) * 1024 + X * 512 + nt * 32 + s * 16 + (lane >> 5) * 8) = o;
}

extern "C" void kernel_launch(void* const* d_in, const int* in_sizes, int n_in, void* d_out, int out_size,
                              void* d_ws, size_t ws_size, hipStream_t stream) {
  (void)in_sizes; (void)n_in; (void)out_size; (void)ws_size;
  (void)hipFuncSetAttribute((const void*)k1_qproj_attn, hipFuncAttributeMaxDynamicSharedMemorySize, K1_LDS);
  (void)hipFuncSetAttribute((const void*)k2_oproj, hipFuncAttributeMaxDynamicSharedMemorySize, K2_LDS);
  const uint16_t* xA = (const uint16_t*)d_in[0];
  const uint16_t* kA = (const uint16_t*)d_in[1];
  const uint16_t* vA = (const uint16_t*)d_in[2];
  const uint16_t* wqA = (const uint16_t*)d_in[3];
  const uint16_t* woA = (const uint16_t*)d_in[4];
  const uint16_t* xB = (const uint16_t*)d_in[5];
  const uint16_t* kB = (const uint16_t*)d_in[6];
  const uint16_t* vB = (const uint16_t*)d_in[7];
  const uint16_t* wqB = (const uint16_t*)d_in[8];
  const uint16_t* woB = (const uint16_t*)d_in[9];
  uint16_t* ows = (uint16_t*)d_ws;
  float* part = (float*)((char*)d_ws + (16u << 20));
  k1_qproj_attn<<<256, 512, K1_LDS, stream>>>(xA, kA, vA, wqA, xB, kB, vB, wqB, ows);
  k2_oproj<<<256, 512, K2_LDS, stream>>>(woA, woB, ows, part);
  k3_reduce<<<256, 512, 0, stream>>>(part, (uint16_t*)d_out);
}
```

```cpp
#include <hip/hip_runtime.h>
#include <hip/hip_bf16.h>
#include <stdint.h>

typedef __attribute__((ext_vector_type(8))) __bf16 bf16x8;
typedef __attribute__((ext_vector_type(2))) __bf16 bf16x2;
typedef __attribute__((ext_vector_type(4))) short s16x4;
typedef __attribute__((ext_vector_type(2))) float f32x2;
typedef __attribute__((ext_vector_type(4))) float f32x4;
typedef __attribute__((ext_vector_type(16))) float f32x16;
typedef __attribute__((ext_vector_type(4))) unsigned u32x4;
typedef __attribute__((address_space(3))) s16x4* lds_s16x4_ptr;

#define DI __device__ __forceinline__
#define MFMA32(a, b, c) __builtin_amdgcn_mfma_f32_32x32x16_bf16((a), (b), (c), 0, 0, 0)
#define MEMFENCE() asm volatile("" ::: "memory")
#define SCHED0() __builtin_amdgcn_sched_barrier(0)

#define NT_KV 1
#define RDFIRST 1
#define USE_PF 0
#define USE_ROT 0
#define NT_WQ 0
#define NT_X 0
#define NT_WO 0

DI unsigned cvtpk(float lo, float hi) {
  f32x2 v = {lo, hi};
  bf16x2 r = __builtin_convertvector(v, bf16x2);
  return __builtin_bit_cast(unsigned, r);
}
DI bf16x8 pack8(const f32x16& x, int s) {
  u32x4 p = {cvtpk(x[8 * s + 0], x[8 * s + 1]), cvtpk(x[8 * s + 2], x[8 * s + 3]),
             cvtpk(x[8 * s + 4], x[8 * s + 5]), cvtpk(x[8 * s + 6], x[8 * s + 7])};
  return __builtin_bit_cast(bf16x8, p);
}
DI s16x4 tr_read(const char* p) { return __builtin_amdgcn_ds_read_tr16_b64_v4i16((lds_s16x4_ptr)(p)); }
DI bf16x8 tr_frag(const char* p0, const char* p1) {
  s16x4 lo = tr_read(p0), hi = tr_read(p1);
  return __builtin_bit_cast(bf16x8, __builtin_shufflevector(lo, hi, 0, 1, 2, 3, 4, 5, 6, 7));
}
template <int NT>
DI void glds16(const void* gbase, unsigned voff, unsigned ldst) {
  unsigned keep;
  if constexpr (NT)
    asm volatile("s_mov_b32 %0, m0\n\ts_mov_b32 m0, %2\n\ts_nop 0\n\tglobal_load_lds_dwordx4 %1, %3 nt\n\ts_mov_b32 m0, %0"
                 : "=&s"(keep) : "v"(voff), "s"(ldst), "s"(gbase) : "memory");
  else
    asm volatile("s_mov_b32 %0, m0\n\ts_mov_b32 m0, %2\n\ts_nop 0\n\tglobal_load_lds_dwordx4 %1, %3\n\ts_mov_b32 m0, %0"
                 : "=&s"(keep) : "v"(voff), "s"(ldst), "s"(gbase) : "memory");
}
DI void pf4(const void* gbase, unsigned voff, unsigned ldst) {
  unsigned keep;
  asm volatile("s_mov_b32 %0, m0\n\ts_mov_b32 m0, %2\n\ts_nop 0\n\tglobal_load_lds_dword %1, %3\n\ts_mov_b32 m0, %0"
               : "=&s"(keep) : "v"(voff), "s"(ldst), "s"(gbase) : "memory");
}
#define PF_STRIDE 128
DI unsigned lds_addr(const void* p) { return (unsigned)(size_t)(__attribute__((address_space(3))) const char*)p; }
template <int A, int B> DI void wait_vm_sel(int cond) {
  asm volatile("s_waitcnt vmcnt(%2) lgkmcnt(0)\n\ts_cmp_eq_u32 %0, 0\n\ts_cbranch_scc1 .Lw%=\n\ts_waitcnt vmcnt(%1)\n\t.Lw%=:"
               ::"s"(cond), "i"(A), "i"(B) : "memory", "scc");
}
#define STAMP(buf, i)
template <int N> DI void wait_vm() { asm volatile("s_waitcnt vmcnt(%0) lgkmcnt(0)" ::"i"(N) : "memory"); }

constexpr int K1_STAGE = 49152;
constexpr int K1_LDS = 3 * K1_STAGE;

__global__ __launch_bounds__(512, 2) void k1_qproj_attn(
    const uint16_t* __restrict__ xA, const uint16_t* __restrict__ kA, const uint16_t* __restrict__ vA,
    const uint16_t* __restrict__ wqA, const uint16_t* __restrict__ xB, const uint16_t* __restrict__ kB,
    const uint16_t* __restrict__ vB, const uint16_t* __restrict__ wqB, uint16_t* __restrict__ ows) {
  extern __shared__ __attribute__((aligned(16))) char smem[];
  const int tid = threadIdx.x, lane = tid & 63;
  const int w = __builtin_amdgcn_readfirstlane(tid >> 6);
  const int bid = blockIdx.x, xcd = bid & 7, idx = bid >> 3;
  const int X = xcd >> 2, h = (xcd & 3) * 8 + (idx & 7), bg = idx >> 3;
  const int b = bg * 8 + w;
  unsigned long long* sb = (unsigned long long*)((char*)ows + (64u << 20)); (void)sb;
  STAMP(sb, 0);
  const uint16_t* x = X ? xB : xA;
  const uint16_t* kc = X ? kB : kA;
  const uint16_t* vc = X ? vB : vA;
  const uint16_t* wq = X ? wqB : wqA;

  const int r = lane & 31, hh = lane >> 5, blk = (lane >> 4) & 1, q = (lane & 15) >> 2, p = lane & 3;

  const int rot = USE_ROT ? ((idx & 7) + 2 * bg) & 7 : 0;
  const char* xb = (const char*)(x + (size_t)(b * 32) * 512);
  const char* wb = (const char*)(wq + (size_t)(w * 8) * 4096 + h * 128);
  const unsigned xoff0 = (lane >> 3) * 1024 + (((lane & 7) ^ (lane >> 4)) << 4);
  const unsigned xoff1 = (8 + (lane >> 3)) * 1024 + (((lane & 7) ^ ((lane >> 4) | 4)) << 4);
  const unsigned woff = (lane >> 4) * 8192 + (((lane & 15) ^ ((lane >> 4) << 2)) << 4);
  const unsigned sbase = lds_addr(smem);

  auto piece = [&](int st, unsigned bufoff, int j) {
    const int ks = (st + rot) & 7;
    const unsigned lb = sbase + bufoff;
    const char* wg = wb + (size_t)ks * (64 * 8192);
    const unsigned lx = lb + 16384 + w * 4096;
    const char* xg = xb + ks * 128;
    if (j == 0) glds16<NT_WQ>(wg, woff, lb + w * 2048);
    if (j == 1) glds16<NT_WQ>(wg + 4 * 8192, woff, lb + w * 2048 + 1024);
    if (j == 2) glds16<NT_X>(xg, xoff0, lx);
    if (j == 3) glds16<NT_X>(xg, xoff1, lx + 1024);
    if (j == 4) glds16<NT_X>(xg + 16384, xoff0, lx + 2048);
    if (j == 5) glds16<NT_X>(xg + 16384, xoff1, lx + 3072);
  };
  auto stage = [&](int st, unsigned bufoff) {
#pragma unroll
    for (int j = 0; j < 6; ++j) piece(st, bufoff, j);
  };
  if (USE_PF) {
    const unsigned scr = sbase + 2 * K1_STAGE + w * 2048;
    const char* px = (const char*)(x + (size_t)(idx * 32) * 512) + w * 4096;
    const char* pw = (const char*)(wq + (size_t)(idx * 16 + w * 2) * 4096 + (xcd & 3) * 1024);
    pf4(px, (lane & 31) * 128 + (lane >> 5) * 0, scr);
    pf4(pw, (lane & 15) * 128 + ((lane >> 4) & 1) * 8192, scr);
  }

  const size_t kvbase = (size_t)(b * 32 + h) * 4096;
  const uint16_t* kb = kc + kvbase;
  const uint16_t* vb = vc + kvbase;
  const unsigned kvo0 = lane * 16, kvo1 = lane * 16 + 4096;
  u32x4 kr[8], vr[8];
#pragma unroll
  for (int i = 0; i < 8; ++i) { kr[i] = u32x4{0, 0, 0, 0}; vr[i] = u32x4{0, 0, 0, 0}; }
#define KVNT " nt"
  auto kvload_if = [&](int cond_) {
    const int cond = __builtin_amdgcn_readfirstlane(cond_);
    asm volatile(
        "s_cmp_eq_u32 %16, 0\n\t"
        "s_cbranch_scc1 .Lkv%=\n\t"
        "global_load_dwordx4 %0, %17, %19" KVNT "\n\t"
        "global_load_dwordx4 %1, %17, %19 offset:1024" KVNT "\n\t"
        "global_load_dwordx4 %2, %17, %19 offset:2048" KVNT "\n\t"
        "global_load_dwordx4 %3, %17, %19 offset:3072" KVNT "\n\t"
        "global_load_dwordx4 %4, %18, %19" KVNT "\n\t"
        "global_load_dwordx4 %5, %18, %19 offset:1024" KVNT "\n\t"
        "global_load_dwordx4 %6, %18, %19 offset:2048" KVNT "\n\t"
        "global_load_dwordx4 %7, %18, %19 offset:3072" KVNT "\n\t"
        "global_load_dwordx4 %8, %17, %20" KVNT "\n\t"
        "global_load_dwordx4 %9, %17, %20 offset:1024" KVNT "\n\t"
        "global_load_dwordx4 %10, %17, %20 offset:2048" KVNT "\n\t"
        "global_load_dwordx4 %11, %17, %20 offset:3072" KVNT "\n\t"
        "global_load_dwordx4 %12, %18, %20" KVNT "\n\t"
        "global_load_dwordx4 %13, %18, %20 offset:1024" KVNT "\n\t"
        "global_load_dwordx4 %14, %18, %20 offset:2048" KVNT "\n\t"
        "global_load_dwordx4 %15, %18, %20 offset:3072" KVNT "\n\t"
        ".Lkv%=:"
        : "+v"(kr[0]), "+v"(kr[1]), "+v"(kr[2]), "+v"(kr[3]), "+v"(kr[4]), "+v"(kr[5]), "+v"(kr[6]), "+v"(kr[7]),
          "+v"(vr[0]), "+v"(vr[1]), "+v"(vr[2]), "+v"(vr[3]), "+v"(vr[4]), "+v"(vr[5]), "+v"(vr[6]), "+v"(vr[7])
        : "s"(cond), "v"(kvo0), "v"(kvo1), "s"(kb), "s"(vb)
        : "memory", "scc");
  };

  f32x16 acc[4];
#pragma unroll
  for (int t = 0; t < 4; ++t)
#pragma unroll
    for (int i = 0; i < 16; ++i) acc[t][i] = 0.f;

  const int troff = (8 * hh + q) * 256 + 32 * blk + 16 * (p & 1) + 8 * (p >> 1);
  const int f = (r >> 1) & 7;
  int tro[4], xo[4];
#pragma unroll
  for (int t = 0; t < 4; ++t) { tro[t] = troff + ((t ^ q) << 6); xo[t] = 16384 + w * 4096 + r * 128 + (((2 * t + hh) ^ f) << 4); }

  bf16x8 af[2][4], bf[2];
  auto ldfrag = [&](const char* lb, int s, int slot) {
    bf[slot] = *(const bf16x8*)(lb + xo[s]);
#pragma unroll
    for (int t = 0; t < 4; ++t) {
      const char* pa = lb + tro[t] + s * 4096;
      af[slot][t] = tr_frag(pa, pa + 1024);
    }
    SCHED0();
  };
  auto mma = [&](int slot, bool doit, int rs, unsigned rb, int j0, int nj) {
    acc[0] = MFMA32(af[slot][0], bf[slot], acc[0]);
    SCHED0();
    if (nj > 0 && doit) piece(rs, rb, j0);
    SCHED0();
    acc[1] = MFMA32(af[slot][1], bf[slot], acc[1]);
    acc[2] = MFMA32(af[slot][2], bf[slot], acc[2]);
    SCHED0();
    if (nj > 1 && doit) piece(rs, rb, j0 + 1);
    SCHED0();
    acc[3] = MFMA32(af[slot][3], bf[slot], acc[3]);
    SCHED0();
  };

#define K1_GRP ((idx >> 0) & 1)
  const int early = __builtin_amdgcn_readfirstlane(K1_GRP);
  kvload_if(early);
  stage(0, 0);
  stage(1, K1_STAGE);
  stage(2, 2 * K1_STAGE);
  wait_vm<12>();
  __builtin_amdgcn_s_barrier();
  MEMFENCE();
  STAMP(sb, 1);
  ldfrag(smem, 0, 0);

  unsigned cur = 0, prv = 0;
#pragma unroll 1
  for (int st = 0; st < 8; ++st) {
    const unsigned nxt = (cur == 2 * K1_STAGE) ? 0u : cur + K1_STAGE;
    const char* lb = smem + cur;
    const bool re = (st >= 1) && (st <= 5);
    ldfrag(lb, 1, 1); mma(0, re, st + 2, prv, 1, 2);
    ldfrag(lb, 2, 0); mma(1, re, st + 2, prv, 3, 2);
    ldfrag(lb, 3, 1); mma(0, re, st + 2, prv, 5, 1);
    kvload_if(st == 5 && !early);
    asm volatile(
        "s_waitcnt vmcnt(22) lgkmcnt(0)\n\t"
        "s_cmp_eq_u32 %0, 5\n\t"
        "s_cbranch_scc1 .Lw%=\n\t"
        "s_waitcnt vmcnt(16)\n\t"
        "s_cmp_ge_u32 %0, 6\n\t"
        "s_cbranch_scc1 .Lw%=\n\t"
        "s_waitcnt vmcnt(6)\n\t"
        "s_cmp_lt_u32 %1, 6\n\t"
        "s_cbranch_scc1 .Lw%=\n\t"
        "s_waitcnt vmcnt(0)\n\t"
        ".Lw%=:" ::"s"(early ? 0 : st), "s"(st) : "memory", "scc");
    __builtin_amdgcn_s_barrier();
    MEMFENCE();
    ldfrag(smem + nxt, 0, 0);
    mma(1, st <= 4, st + 3, cur, 0, 1);
    prv = cur;
    cur = nxt;
  }

  STAMP(sb, 2);
  char* vl = smem + (w < 6 ? w * 8192 : 2 * K1_STAGE + (w - 6) * 8192);
  asm volatile("s_waitcnt vmcnt(8)" : "+v"(kr[0]), "+v"(kr[1]), "+v"(kr[2]), "+v"(kr[3]), "+v"(kr[4]), "+v"(kr[5]), "+v"(kr[6]), "+v"(kr[7])::"memory");
#pragma unroll
  for (int i = 0; i < 8; ++i)
    *(u32x4*)(vl + i * 1024 + (lane >> 4) * 256 + (((lane & 15) ^ ((4 * i + (lane >> 4)) & 15)) << 4)) = kr[i];
  const int pr = (r & 0x13) | ((r & 4) << 1) | ((r & 8) >> 1);
  bf16x8 kf[8];
#pragma unroll
  for (int i = 0; i < 8; ++i) kf[i] = *(const bf16x8*)(vl + pr * 256 + (((2 * i + hh) ^ (pr & 15)) << 4));
  asm volatile("s_waitcnt vmcnt(0)" : "+v"(vr[0]), "+v"(vr[1]), "+v"(vr[2]), "+v"(vr[3]), "+v"(vr[4]), "+v"(vr[5]), "+v"(vr[6]), "+v"(vr[7])::"memory");
#pragma unroll
  for (int i = 0; i < 8; ++i)
    *(u32x4*)(vl + i * 1024 + (lane >> 4) * 256 + (((lane & 15) ^ ((lane >> 4) << 2)) << 4)) = vr[i];

  f32x16 sacc;
#pragma unroll
  for (int i = 0; i < 16; ++i) sacc[i] = 0.f;
#pragma unroll
  for (int t = 0; t < 4; ++t)
#pragma unroll
    for (int s = 0; s < 2; ++s)
      sacc = MFMA32(kf[2 * t + s], pack8(acc[t], s), sacc);

  STAMP(sb, 3);
  const float scale = 0.08838834764831845f;
  float m = -3.0e38f;
#pragma unroll
  for (int i = 0; i < 16; i += 2) {
    unsigned u = cvtpk(sacc[i], sacc[i + 1]);
    float a = __uint_as_float(u << 16) * scale, c = __uint_as_float(u & 0xffff0000u) * scale;
    unsigned u2 = cvtpk(a, c);
    sacc[i] = __uint_as_float(u2 << 16);
    sacc[i + 1] = __uint_as_float(u2 & 0xffff0000u);
    m = fmaxf(m, fmaxf(sacc[i], sacc[i + 1]));
  }
  {
    auto rr = __builtin_amdgcn_permlane32_swap(__float_as_uint(m), __float_as_uint(m), false, false);
    m = fmaxf(__uint_as_float(rr[0]), __uint_as_float(rr[1]));
  }
  float sum = 0.f;
#pragma unroll
  for (int i = 0; i < 16; ++i) {
    sacc[i] = __builtin_amdgcn_exp2f((sacc[i] - m) * 1.4426950408889634f);
    sum += sacc[i];
  }
  {
    auto rr = __builtin_amdgcn_permlane32_swap(__float_as_uint(sum), __float_as_uint(sum), false, false);
    sum = __uint_as_float(rr[0]) + __uint_as_float(rr[1]);
  }
  const float inv = 1.0f / sum;
#pragma unroll
  for (int i = 0; i < 16; ++i) sacc[i] *= inv;
  bf16x8 pf0 = pack8(sacc, 0), pf1 = pack8(sacc, 1);

  u32x4* od = (u32x4*)(ows + ((size_t)((X * 32 + b) * 256 + h * 8) * 64 + lane) * 8);
#pragma unroll
  for (int t = 0; t < 4; ++t) {
    f32x16 o;
#pragma unroll
    for (int i = 0; i < 16; ++i) o[i] = 0.f;
    const char* pa = vl + tro[t];
    o = MFMA32(tr_frag(pa, pa + 1024), pf0, o);
    o = MFMA32(tr_frag(pa + 4096, pa + 4096 + 1024), pf1, o);
    od[(2 * t) * 64] = __builtin_bit_cast(u32x4, pack8(o, 0));
    od[(2 * t + 1) * 64] = __builtin_bit_cast(u32x4, pack8(o, 1));
  }
  STAMP(sb, 4);
  STAMP(sb, 5);
}

constexpr int K2_LDS = 131072;

__global__ __launch_bounds__(512, 2) void k2_oproj(const uint16_t* __restrict__ woA, const uint16_t* __restrict__ woB,
                                                   const uint16_t* __restrict__ ows, uint16_t* __restrict__ out) {
  extern __shared__ __attribute__((aligned(16))) char smem[];
  const int tid = threadIdx.x, lane = tid & 63;
  const int w = __builtin_amdgcn_readfirstlane(tid >> 6);
  const int bid = blockIdx.x, xcd = bid & 7, idx = bid >> 3;
  const int X = xcd >> 2, rp = (xcd & 3) * 4 + (idx >> 3), nc = idx & 7;
  const uint16_t* wo = X ? woB : woA;
  unsigned long long* sb = (unsigned long long*)((char*)ows + (65u << 20)); (void)sb;
  STAMP(sb, 0);
  const int r = lane & 31, hh = lane >> 5, blk = (lane >> 4) & 1, q = (lane & 15) >> 2, p = lane & 3;

  const char* wb = (const char*)(wo + (size_t)(w * 512) * 512 + nc * 64);
  const unsigned woff = (lane >> 3) * 1024 + (((lane & 7) ^ (((lane >> 4) & 1) << 2)) << 4);
  const char* lw = smem + w * 16384;
  const unsigned lwa = lds_addr(smem) + w * 16384;
  const char* ob = (const char*)ows + (size_t)((X * 32 + rp * 2) * 256 + w * 32) * 1024;
  const unsigned oo0 = lane * 16, oo1 = lane * 16 + 262144;

  u32x4 of[8][2];
  f32x16 acc[2][2];
#pragma unroll
  for (int a = 0; a < 2; ++a)
#pragma unroll
    for (int c = 0; c < 2; ++c)
#pragma unroll
      for (int i = 0; i < 16; ++i) acc[a][c][i] = 0.f;

  auto piece = [&](int ks, int j) {
    const int slot = ks & 7;
    if (j == 0) glds16<NT_WO>(wb + (size_t)ks * 16384, woff, lwa + slot * 2048);
    if (j == 1) glds16<NT_WO>(wb + (size_t)ks * 16384 + 8192, woff, lwa + slot * 2048 + 1024);
    if (j == 2) asm volatile("global_load_dwordx4 %0, %1, %2" : "=v"(of[slot][0]) : "v"(oo0), "s"(ob + ks * 1024) : "memory");
    if (j == 3) asm volatile("global_load_dwordx4 %0, %1, %2" : "=v"(of[slot][1]) : "v"(oo1), "s"(ob + ks * 1024) : "memory");
  };

  const int troff = (8 * hh + q) * 128 + 32 * blk + 16 * (p & 1) + 8 * (p >> 1);
  const int hsw = (q >> 1) & 1;
  const char* pa0 = lw + troff + ((0 ^ hsw) << 6);
  const char* pa1 = lw + troff + ((1 ^ hsw) << 6);
  bf16x8 af[2][2];
  auto ldfrag = [&](int ks) {
    const int o = (ks & 7) * 2048;
    af[ks & 1][0] = tr_frag(pa0 + o, pa0 + o + 512);
    af[ks & 1][1] = tr_frag(pa1 + o, pa1 + o + 512);
  };
#define K2_WAIT(N, ks) asm volatile("s_waitcnt vmcnt(%2)" : "+v"(of[(ks) & 7][0]), "+v"(of[(ks) & 7][1]) : "i"(N) : "memory")

#pragma unroll
  for (int ks = 0; ks < 7; ++ks)
#pragma unroll
    for (int j = 0; j < 4; ++j) piece(ks, j);
  K2_WAIT(24, 0);
  STAMP(sb, 1);
  ldfrag(0);

#pragma unroll
  for (int ks = 0; ks < 32; ++ks) {
    if (ks < 31) {
      constexpr int dummy = 0; (void)dummy;
      const int last = (ks + 6 < 31) ? ks + 6 : 31;
      switch (last - (ks + 1)) {
        case 5: K2_WAIT(20, ks + 1); break;
        case 4: K2_WAIT(16, ks + 1); break;
        case 3: K2_WAIT(12, ks + 1); break;
        case 2: K2_WAIT(8, ks + 1); break;
        case 1: K2_WAIT(4, ks + 1); break;
        default: K2_WAIT(0, ks + 1); break;
      }
    }
    const bf16x8 b0 = __builtin_bit_cast(bf16x8, of[ks & 7][0]), b1 = __builtin_bit_cast(bf16x8, of[ks & 7][1]);
    const bool pre = ks + 7 < 32;
    acc[0][0] = MFMA32(af[ks & 1][0], b0, acc[0][0]);
    SCHED0();
    if (ks < 31) ldfrag(ks + 1);
    if (pre) piece(ks + 7, 0);
    SCHED0();
    acc[0][1] = MFMA32(af[ks & 1][0], b1, acc[0][1]);
    SCHED0();
    if (pre) piece(ks + 7, 1);
    SCHED0();
    acc[1][0] = MFMA32(af[ks & 1][1], b0, acc[1][0]);
    SCHED0();
    if (pre) piece(ks + 7, 2);
    SCHED0();
    acc[1][1] = MFMA32(af[ks & 1][1], b1, acc[1][1]);
    SCHED0();
    if (pre) piece(ks + 7, 3);
    SCHED0();
  }
  STAMP(sb, 2);

  {
    f32x4* rd = (f32x4*)(smem + w * 16384) + lane;
#pragma unroll
    for (int nt = 0; nt < 2; ++nt)
#pragma unroll
      for (int bt = 0; bt < 2; ++bt)
#pragma unroll
        for (int g = 0; g < 4; ++g) {
          f32x4 v = {acc[nt][bt][4 * g], acc[nt][bt][4 * g + 1], acc[nt][bt][4 * g + 2], acc[nt][bt][4 * g + 3]};
          rd[((nt * 2 + bt) * 4 + g) * 64] = v;
        }
  }
  __syncthreads();
  {
    const f32x4* rs = (const f32x4*)smem + (w * 2) * 64 + lane;
    f32x4 s0 = rs[0], s1 = rs[64];
#pragma unroll
    for (int j = 1; j < 8; ++j) {
      s0 += rs[j * 1024];
      s1 += rs[j * 1024 + 64];
    }
    const int tile = w >> 1, nt = tile >> 1, bt = tile & 1, s = w & 1;
    u32x4 o = {cvtpk(s0[0], s0[1]), cvtpk(s0[2], s0[3]), cvtpk(s1[0], s1[1]), cvtpk(s1[2], s1[3])};
    uint16_t* dst = out + (size_t)(rp * 64 + bt * 32 + r) * 1024 + X * 512 + nc * 64 + nt * 32 + s * 16 + hh * 8;
    *(u32x4*)dst = o;
  }
  STAMP(sb, 4);
  STAMP(sb, 5);
}

extern "C" void kernel_launch(void* const* d_in, const int* in_sizes, int n_in, void* d_out, int out_size,
                              void* d_ws, size_t ws_size, hipStream_t stream) {
  (void)in_sizes; (void)n_in; (void)out_size; (void)ws_size;
  (void)hipFuncSetAttribute((const void*)k1_qproj_attn, hipFuncAttributeMaxDynamicSharedMemorySize, K1_LDS);
  (void)hipFuncSetAttribute((const void*)k2_oproj, hipFuncAttributeMaxDynamicSharedMemorySize, K2_LDS);
  const uint16_t* xA = (const uint16_t*)d_in[0];
  const uint16_t* kA = (const uint16_t*)d_in[1];
  const uint16_t* vA = (const uint16_t*)d_in[2];
  const uint16_t* wqA = (const uint16_t*)d_in[3];
  const uint16_t* woA = (const uint16_t*)d_in[4];
  const uint16_t* xB = (const uint16_t*)d_in[5];
  const uint16_t* kB = (const uint16_t*)d_in[6];
  const uint16_t* vB = (const uint16_t*)d_in[7];
  const uint16_t* wqB = (const uint16_t*)d_in[8];
  const uint16_t* woB = (const uint16_t*)d_in[9];
  uint16_t* ows = (uint16_t*)d_ws;
  k1_qproj_attn<<<256, 512, K1_LDS, stream>>>(xA, kA, vA, wqA, xB, kB, vB, wqB, ows);
  k2_oproj<<<256, 512, K2_LDS, stream>>>(woA, woB, ows, (uint16_t*)d_out);
}
```

```cpp
#include <hip/hip_runtime.h>
#include <hip/hip_bf16.h>
#include <stdint.h>

typedef __attribute__((ext_vector_type(8))) __bf16 bf16x8;
typedef __attribute__((ext_vector_type(2))) __bf16 bf16x2;
typedef __attribute__((ext_vector_type(4))) short s16x4;
typedef __attribute__((ext_vector_type(2))) float f32x2;
typedef __attribute__((ext_vector_type(4))) float f32x4;
typedef __attribute__((ext_vector_type(16))) float f32x16;
typedef __attribute__((ext_vector_type(4))) unsigned u32x4;
typedef __attribute__((address_space(3))) s16x4* lds_s16x4_ptr;

#define DI __device__ __forceinline__
#define MFMA32(a, b, c) __builtin_amdgcn_mfma_f32_32x32x16_bf16((a), (b), (c), 0, 0, 0)
#define MEMFENCE() asm volatile("" ::: "memory")
#define SCHED0() __builtin_amdgcn_sched_barrier(0)
#define USE_PRIO 1
#define PRIO(n) __builtin_amdgcn_s_setprio(n)

#define NT_KV 1
#define RDFIRST 1
#define PF_WO 2
#define EARLY_RD true
#define K1_ILV 1
#define PF_WO_ST 7
#define USE_PF 0
#define USE_ROT 0
#define NT_WQ 0
#define NT_X 0
#define NT_WO 0

DI unsigned cvtpk(float lo, float hi) {
  f32x2 v = {lo, hi};
  bf16x2 r = __builtin_convertvector(v, bf16x2);
  return __builtin_bit_cast(unsigned, r);
}
DI bf16x8 pack8(const f32x16& x, int s) {
  u32x4 p = {cvtpk(x[8 * s + 0], x[8 * s + 1]), cvtpk(x[8 * s + 2], x[8 * s + 3]),
             cvtpk(x[8 * s + 4], x[8 * s + 5]), cvtpk(x[8 * s + 6], x[8 * s + 7])};
  return __builtin_bit_cast(bf16x8, p);
}
DI s16x4 tr_read(const char* p) { return __builtin_amdgcn_ds_read_tr16_b64_v4i16((lds_s16x4_ptr)(p)); }
DI bf16x8 tr_frag(const char* p0, const char* p1) {
  s16x4 lo = tr_read(p0), hi = tr_read(p1);
  return __builtin_bit_cast(bf16x8, __builtin_shufflevector(lo, hi, 0, 1, 2, 3, 4, 5, 6, 7));
}
template <int NT>
DI void glds16(const void* gbase, unsigned voff, unsigned ldst) {
  unsigned keep;
  if constexpr (NT)
    asm volatile("s_mov_b32 %0, m0\n\ts_mov_b32 m0, %2\n\ts_nop 0\n\tglobal_load_lds_dwordx4 %1, %3 nt\n\ts_mov_b32 m0, %0"
                 : "=&s"(keep) : "v"(voff), "s"(ldst), "s"(gbase) : "memory");
  else
    asm volatile("s_mov_b32 %0, m0\n\ts_mov_b32 m0, %2\n\ts_nop 0\n\tglobal_load_lds_dwordx4 %1, %3\n\ts_mov_b32 m0, %0"
                 : "=&s"(keep) : "v"(voff), "s"(ldst), "s"(gbase) : "memory");
}
DI void pf4(const void* gbase, unsigned voff, unsigned ldst) {
  unsigned keep;
  asm volatile("s_mov_b32 %0, m0\n\ts_mov_b32 m0, %2\n\ts_nop 0\n\tglobal_load_lds_dword %1, %3\n\ts_mov_b32 m0, %0"
               : "=&s"(keep) : "v"(voff), "s"(ldst), "s"(gbase) : "memory");
}
#define PF_STRIDE 128
DI unsigned lds_addr(const void* p) { return (unsigned)(size_t)(__attribute__((address_space(3))) const char*)p; }
template <int A, int B> DI void wait_vm_sel(int cond) {
  asm volatile("s_waitcnt vmcnt(%2) lgkmcnt(0)\n\ts_cmp_eq_u32 %0, 0\n\ts_cbranch_scc1 .Lw%=\n\ts_waitcnt vmcnt(%1)\n\t.Lw%=:"
               ::"s"(cond), "i"(A), "i"(B) : "memory", "scc");
}
#define STAMP(buf, i)
template <int N> DI void wait_vm() { asm volatile("s_waitcnt vmcnt(%0) lgkmcnt(0)" ::"i"(N) : "memory"); }

constexpr int K1_STAGE = 49152;
constexpr int K1_LDS = 3 * K1_STAGE + 2048;

__global__ __launch_bounds__(512, 2) void k1_qproj_attn(
    const uint16_t* __restrict__ xA, const uint16_t* __restrict__ kA, const uint16_t* __restrict__ vA,
    const uint16_t* __restrict__ wqA, const uint16_t* __restrict__ xB, const uint16_t* __restrict__ kB,
    const uint16_t* __restrict__ vB, const uint16_t* __restrict__ wqB, uint16_t* __restrict__ ows,
    const uint16_t* __restrict__ woA, const uint16_t* __restrict__ woB) {
  extern __shared__ __attribute__((aligned(16))) char smem[];
  const int tid = threadIdx.x, lane = tid & 63;
  const int w = __builtin_amdgcn_readfirstlane(tid >> 6);
  const int bid = blockIdx.x, xcd = bid & 7, idx = bid >> 3;
  const int X = xcd >> 2, h = (xcd & 3) * 8 + (idx & 7), bg = idx >> 3;
  const int b = bg * 8 + w;
  unsigned long long* sb = (unsigned long long*)((char*)ows + (64u << 20)); (void)sb;
  STAMP(sb, 0);
  const uint16_t* x = X ? xB : xA;
  const uint16_t* kc = X ? kB : kA;
  const uint16_t* vc = X ? vB : vA;
  const uint16_t* wq = X ? wqB : wqA;

  const int r = lane & 31, hh = lane >> 5, blk = (lane >> 4) & 1, q = (lane & 15) >> 2, p = lane & 3;

  const int rot = USE_ROT ? ((idx & 7) + 2 * bg) & 7 : 0;
  const char* xb = (const char*)(x + (size_t)(b * 32) * 512);
  const char* wb = (const char*)(wq + (size_t)(w * 8) * 4096 + h * 128);
  const unsigned xoff0 = (lane >> 3) * 1024 + (((lane & 7) ^ (lane >> 4)) << 4);
  const unsigned xoff1 = (8 + (lane >> 3)) * 1024 + (((lane & 7) ^ ((lane >> 4) | 4)) << 4);
  const unsigned woff = (lane >> 4) * 8192 + (((lane & 15) ^ ((lane >> 4) << 2)) << 4);
  const unsigned sbase = lds_addr(smem);

  auto piece = [&](int st, unsigned bufoff, int j) {
    const int ks = (st + rot) & 7;
    const unsigned lb = sbase + bufoff;
    const char* wg = wb + (size_t)ks * (64 * 8192);
    const unsigned lx = lb + 16384 + w * 4096;
    const char* xg = xb + ks * 128;
    if (j == 0) glds16<NT_WQ>(wg, woff, lb + w * 2048);
    if (j == 1) glds16<NT_WQ>(wg + 4 * 8192, woff, lb + w * 2048 + 1024);
    if (j == 2) glds16<NT_X>(xg, xoff0, lx);
    if (j == 3) glds16<NT_X>(xg, xoff1, lx + 1024);
    if (j == 4) glds16<NT_X>(xg + 16384, xoff0, lx + 2048);
    if (j == 5) glds16<NT_X>(xg + 16384, xoff1, lx + 3072);
  };
  auto stage = [&](int st, unsigned bufoff) {
#pragma unroll
    for (int j = 0; j < 6; ++j) piece(st, bufoff, j);
  };
  if (USE_PF) {
    const unsigned scr = sbase + 2 * K1_STAGE + w * 2048;
    const char* px = (const char*)(x + (size_t)(idx * 32) * 512) + w * 4096;
    const char* pw = (const char*)(wq + (size_t)(idx * 16 + w * 2) * 4096 + (xcd & 3) * 1024);
    pf4(px, (lane & 31) * 128 + (lane >> 5) * 0, scr);
    pf4(pw, (lane & 15) * 128 + ((lane >> 4) & 1) * 8192, scr);
  }

  const size_t kvbase = (size_t)(b * 32 + h) * 4096;
  const uint16_t* kb = kc + kvbase;
  const uint16_t* vb = vc + kvbase;
  const unsigned kvo0 = lane * 16, kvo1 = lane * 16 + 4096;
  u32x4 kr[8], vr[8];
#pragma unroll
  for (int i = 0; i < 8; ++i) { kr[i] = u32x4{0, 0, 0, 0}; vr[i] = u32x4{0, 0, 0, 0}; }
#define KVNT " nt"
  auto kvload_if = [&](int cond_) {
    const int cond = __builtin_amdgcn_readfirstlane(cond_);
    asm volatile(
        "s_cmp_eq_u32 %16, 0\n\t"
        "s_cbranch_scc1 .Lkv%=\n\t"
        "global_load_dwordx4 %0, %17, %19" KVNT "\n\t"
        "global_load_dwordx4 %1, %17, %19 offset:1024" KVNT "\n\t"
        "global_load_dwordx4 %2, %17, %19 offset:2048" KVNT "\n\t"
        "global_load_dwordx4 %3, %17, %19 offset:3072" KVNT "\n\t"
        "global_load_dwordx4 %4, %18, %19" KVNT "\n\t"
        "global_load_dwordx4 %5, %18, %19 offset:1024" KVNT "\n\t"
        "global_load_dwordx4 %6, %18, %19 offset:2048" KVNT "\n\t"
        "global_load_dwordx4 %7, %18, %19 offset:3072" KVNT "\n\t"
        "global_load_dwordx4 %8, %17, %20" KVNT "\n\t"
        "global_load_dwordx4 %9, %17, %20 offset:1024" KVNT "\n\t"
        "global_load_dwordx4 %10, %17, %20 offset:2048" KVNT "\n\t"
        "global_load_dwordx4 %11, %17, %20 offset:3072" KVNT "\n\t"
        "global_load_dwordx4 %12, %18, %20" KVNT "\n\t"
        "global_load_dwordx4 %13, %18, %20 offset:1024" KVNT "\n\t"
        "global_load_dwordx4 %14, %18, %20 offset:2048" KVNT "\n\t"
        "global_load_dwordx4 %15, %18, %20 offset:3072" KVNT "\n\t"
        ".Lkv%=:"
        : "+v"(kr[0]), "+v"(kr[1]), "+v"(kr[2]), "+v"(kr[3]), "+v"(kr[4]), "+v"(kr[5]), "+v"(kr[6]), "+v"(kr[7]),
          "+v"(vr[0]), "+v"(vr[1]), "+v"(vr[2]), "+v"(vr[3]), "+v"(vr[4]), "+v"(vr[5]), "+v"(vr[6]), "+v"(vr[7])
        : "s"(cond), "v"(kvo0), "v"(kvo1), "s"(kb), "s"(vb)
        : "memory", "scc");
  };

  f32x16 acc[4];
#pragma unroll
  for (int t = 0; t < 4; ++t)
#pragma unroll
    for (int i = 0; i < 16; ++i) acc[t][i] = 0.f;

  const int troff = (8 * hh + q) * 256 + 32 * blk + 16 * (p & 1) + 8 * (p >> 1);
  const int f = (r >> 1) & 7;
  int tro[4], xo[4];
#pragma unroll
  for (int t = 0; t < 4; ++t) { tro[t] = troff + ((t ^ q) << 6); xo[t] = 16384 + w * 4096 + r * 128 + (((2 * t + hh) ^ f) << 4); }

  bf16x8 af[2][4], bf[2];
  auto ldfrag = [&](const char* lb, int s, int slot) {
    bf[slot] = *(const bf16x8*)(lb + xo[s]);
#pragma unroll
    for (int t = 0; t < 4; ++t) {
      const char* pa = lb + tro[t] + s * 4096;
      af[slot][t] = tr_frag(pa, pa + 1024);
    }
    SCHED0();
  };
  auto kstep = [&](int slot, const char* lb, int s, bool doit, int rs, unsigned rb, int j0, int nj, bool early_rd) {
    const int ns = slot ^ 1;
    acc[0] = MFMA32(af[slot][0], bf[slot], acc[0]);
    SCHED0();
    bf[ns] = *(const bf16x8*)(lb + xo[s]);
    af[ns][0] = tr_frag(lb + tro[0] + s * 4096, lb + tro[0] + s * 4096 + 1024);
    if (early_rd) {
      af[ns][1] = tr_frag(lb + tro[1] + s * 4096, lb + tro[1] + s * 4096 + 1024);
      af[ns][2] = tr_frag(lb + tro[2] + s * 4096, lb + tro[2] + s * 4096 + 1024);
      af[ns][3] = tr_frag(lb + tro[3] + s * 4096, lb + tro[3] + s * 4096 + 1024);
    }
    SCHED0();
    if (nj > 0 && doit && !early_rd) piece(rs, rb, j0);
    SCHED0();
    acc[1] = MFMA32(af[slot][1], bf[slot], acc[1]);
    SCHED0();
    if (!early_rd) {
      af[ns][1] = tr_frag(lb + tro[1] + s * 4096, lb + tro[1] + s * 4096 + 1024);
      af[ns][2] = tr_frag(lb + tro[2] + s * 4096, lb + tro[2] + s * 4096 + 1024);
    }
    if (nj > 0 && doit && early_rd) piece(rs, rb, j0);
    SCHED0();
    acc[2] = MFMA32(af[slot][2], bf[slot], acc[2]);
    SCHED0();
    if (!early_rd) af[ns][3] = tr_frag(lb + tro[3] + s * 4096, lb + tro[3] + s * 4096 + 1024);
    SCHED0();
    if (nj > 1 && doit) piece(rs, rb, j0 + 1);
    SCHED0();
    acc[3] = MFMA32(af[slot][3], bf[slot], acc[3]);
    SCHED0();
  };

#define K1_GRP ((idx >> 0) & 1)
  const int early = __builtin_amdgcn_readfirstlane(K1_GRP);
  auto pfwo = [&]() {
    const char* pw = (const char*)((bid & 1) ? woB : woA) + (size_t)(bid >> 1) * 32768 + w * 4096;
    pf4(pw, lane * 64, sbase + 3 * K1_STAGE + w * 256);
  };
  if (PF_WO == 1) pfwo();
  kvload_if(early);
  stage(0, 0);
  stage(1, K1_STAGE);
  stage(2, 2 * K1_STAGE);
  wait_vm<12>();
  __builtin_amdgcn_s_barrier();
  MEMFENCE();
  STAMP(sb, 1);
  ldfrag(smem, 0, 0);

  unsigned cur = 0, prv = 0;
#pragma unroll 1
  for (int st = 0; st < 8; ++st) {
    const unsigned nxt = (cur == 2 * K1_STAGE) ? 0u : cur + K1_STAGE;
    const char* lb = smem + cur;
    const bool re = (st >= 1) && (st <= 5);
    PRIO(2);
    kstep(0, lb, 1, re, st + 2, prv, 1, 2, false);
    PRIO(1);
    kstep(1, lb, 2, re, st + 2, prv, 3, 2, false);
    PRIO(0);
    kstep(0, lb, 3, re, st + 2, prv, 5, 1, EARLY_RD);
    kvload_if(st == 5 && !early);

    asm volatile(
        "s_waitcnt vmcnt(22) lgkmcnt(0)\n\t"
        "s_cmp_eq_u32 %0, 5\n\t"
        "s_cbranch_scc1 .Lw%=\n\t"
        "s_waitcnt vmcnt(16)\n\t"
        "s_cmp_ge_u32 %0, 6\n\t"
        "s_cbranch_scc1 .Lw%=\n\t"
        "s_waitcnt vmcnt(6)\n\t"
        "s_cmp_lt_u32 %1, 6\n\t"
        "s_cbranch_scc1 .Lw%=\n\t"
        "s_waitcnt vmcnt(0)\n\t"
        ".Lw%=:" ::"s"(early ? 0 : st), "s"(st) : "memory", "scc");
    __builtin_amdgcn_s_barrier();
    MEMFENCE();
    PRIO(3);
    kstep(1, smem + nxt, 0, st <= 4, st + 3, cur, 0, 1, false);
    prv = cur;
    cur = nxt;
  }

  STAMP(sb, 2);
  char* vl = smem + (w < 6 ? w * 8192 : 2 * K1_STAGE + (w - 6) * 8192);
  asm volatile("s_waitcnt vmcnt(8)" : "+v"(kr[0]), "+v"(kr[1]), "+v"(kr[2]), "+v"(kr[3]), "+v"(kr[4]), "+v"(kr[5]), "+v"(kr[6]), "+v"(kr[7])::"memory");
#pragma unroll
  for (int i = 0; i < 8; ++i)
    *(u32x4*)(vl + i * 1024 + (lane >> 4) * 256 + (((lane & 15) ^ ((4 * i + (lane >> 4)) & 15)) << 4)) = kr[i];
  const int pr = (r & 0x13) | ((r & 4) << 1) | ((r & 8) >> 1);
  bf16x8 kf[8];
#pragma unroll
  for (int i = 0; i < 8; ++i) kf[i] = *(const bf16x8*)(vl + pr * 256 + (((2 * i + hh) ^ (pr & 15)) << 4));
  asm volatile("s_waitcnt vmcnt(0)" : "+v"(vr[0]), "+v"(vr[1]), "+v"(vr[2]), "+v"(vr[3]), "+v"(vr[4]), "+v"(vr[5]), "+v"(vr[6]), "+v"(vr[7])::"memory");
#pragma unroll
  for (int i = 0; i < 8; ++i)
    *(u32x4*)(vl + i * 1024 + (lane >> 4) * 256 + (((lane & 15) ^ ((lane >> 4) << 2)) << 4)) = vr[i];

  if (PF_WO == 2) pfwo();
  f32x16 sacc;
#pragma unroll
  for (int i = 0; i < 16; ++i) sacc[i] = 0.f;
#pragma unroll
  for (int t = 0; t < 4; ++t)
#pragma unroll
    for (int s = 0; s < 2; ++s)
      sacc = MFMA32(kf[2 * t + s], pack8(acc[t], s), sacc);

  STAMP(sb, 3);
  const float scale = 0.08838834764831845f;
  float m = -3.0e38f;
#pragma unroll
  for (int i = 0; i < 16; i += 2) {
    unsigned u = cvtpk(sacc[i], sacc[i + 1]);
    float a = __uint_as_float(u << 16) * scale, c = __uint_as_float(u & 0xffff0000u) * scale;
    unsigned u2 = cvtpk(a, c);
    sacc[i] = __uint_as_float(u2 << 16);
    sacc[i + 1] = __uint_as_float(u2 & 0xffff0000u);
    m = fmaxf(m, fmaxf(sacc[i], sacc[i + 1]));
  }
  {
    auto rr = __builtin_amdgcn_permlane32_swap(__float_as_uint(m), __float_as_uint(m), false, false);
    m = fmaxf(__uint_as_float(rr[0]), __uint_as_float(rr[1]));
  }
  float sum = 0.f;
#pragma unroll
  for (int i = 0; i < 16; ++i) {
    sacc[i] = __builtin_amdgcn_exp2f((sacc[i] - m) * 1.4426950408889634f);
    sum += sacc[i];
  }
  {
    auto rr = __builtin_amdgcn_permlane32_swap(__float_as_uint(sum), __float_as_uint(sum), false, false);
    sum = __uint_as_float(rr[0]) + __uint_as_float(rr[1]);
  }
  const float inv = 1.0f / sum;
#pragma unroll
  for (int i = 0; i < 16; ++i) sacc[i] *= inv;
  bf16x8 pf0 = pack8(sacc, 0), pf1 = pack8(sacc, 1);

  u32x4* od = (u32x4*)(ows + ((size_t)((X * 32 + b) * 256 + h * 8) * 64 + lane) * 8);
#pragma unroll
  for (int t = 0; t < 4; ++t) {
    f32x16 o;
#pragma unroll
    for (int i = 0; i < 16; ++i) o[i] = 0.f;
    const char* pa = vl + tro[t];
    o = MFMA32(tr_frag(pa, pa + 1024), pf0, o);
    o = MFMA32(tr_frag(pa + 4096, pa + 4096 + 1024), pf1, o);
    od[(2 * t) * 64] = __builtin_bit_cast(u32x4, pack8(o, 0));
    od[(2 * t + 1) * 64] = __builtin_bit_cast(u32x4, pack8(o, 1));
  }
  STAMP(sb, 4);
  STAMP(sb, 5);
}

constexpr int K2_LDS = 131072;

__global__ __launch_bounds__(512, 2) void k2_oproj(const uint16_t* __restrict__ woA, const uint16_t* __restrict__ woB,
                                                   const uint16_t* __restrict__ ows, uint16_t* __restrict__ out) {
  extern __shared__ __attribute__((aligned(16))) char smem[];
  const int tid = threadIdx.x, lane = tid & 63;
  const int w = __builtin_amdgcn_readfirstlane(tid >> 6);
  const int bid = blockIdx.x, xcd = bid & 7, idx = bid >> 3;
  const int X = xcd >> 2, rp = (xcd & 3) * 4 + (idx >> 3), nc = idx & 7;
  const uint16_t* wo = X ? woB : woA;
  unsigned long long* sb = (unsigned long long*)((char*)ows + (65u << 20)); (void)sb;
  STAMP(sb, 0);
  const int r = lane & 31, hh = lane >> 5, blk = (lane >> 4) & 1, q = (lane & 15) >> 2, p = lane & 3;

  const char* wb = (const char*)(wo + (size_t)(w * 512) * 512 + nc * 64);
  const unsigned woff = (lane >> 3) * 1024 + (((lane & 7) ^ (((lane >> 4) & 1) << 2)) << 4);
  const char* lw = smem + w * 16384;
  const unsigned lwa = lds_addr(smem) + w * 16384;
  const char* ob = (const char*)ows + (size_t)((X * 32 + rp * 2) * 256 + w * 32) * 1024;
  const unsigned oo0 = lane * 16, oo1 = lane * 16 + 262144;

  u32x4 of[8][2];
  f32x16 acc[2][2];
#pragma unroll
  for (int a = 0; a < 2; ++a)
#pragma unroll
    for (int c = 0; c < 2; ++c)
#pragma unroll
      for (int i = 0; i < 16; ++i) acc[a][c][i] = 0.f;

#define K2_ROT 0
  const int rot = K2_ROT;
  auto piece = [&](int ks_, int j) {
    const int slot = ks_ & 7, ks = (ks_ + rot) & 31;
    if (j == 0) glds16<NT_WO>(wb + (size_t)ks * 16384, woff, lwa + slot * 2048);
    if (j == 1) glds16<NT_WO>(wb + (size_t)ks * 16384 + 8192, woff, lwa + slot * 2048 + 1024);
    if (j == 2) asm volatile("global_load_dwordx4 %0, %1, %2" : "=v"(of[slot][0]) : "v"(oo0), "s"(ob + ks * 1024) : "memory");
    if (j == 3) asm volatile("global_load_dwordx4 %0, %1, %2" : "=v"(of[slot][1]) : "v"(oo1), "s"(ob + ks * 1024) : "memory");
  };

  const int troff = (8 * hh + q) * 128 + 32 * blk + 16 * (p & 1) + 8 * (p >> 1);
  const int hsw = (q >> 1) & 1;
  const char* pa0 = lw + troff + ((0 ^ hsw) << 6);
  const char* pa1 = lw + troff + ((1 ^ hsw) << 6);
  bf16x8 af[2][2];
  auto ldfrag = [&](int ks) {
    const int o = (ks & 7) * 2048;
    af[ks & 1][0] = tr_frag(pa0 + o, pa0 + o + 512);
    af[ks & 1][1] = tr_frag(pa1 + o, pa1 + o + 512);
  };
#define K2_WAIT(N, ks) asm volatile("s_waitcnt vmcnt(%2)" : "+v"(of[(ks) & 7][0]), "+v"(of[(ks) & 7][1]) : "i"(N) : "memory")

#pragma unroll
  for (int ks = 0; ks < 7; ++ks)
#pragma unroll
    for (int j = 0; j < 4; ++j) piece(ks, j);
  K2_WAIT(24, 0);
  STAMP(sb, 1);
  ldfrag(0);

#pragma unroll
  for (int ks = 0; ks < 32; ++ks) {
    if (ks < 31) {
      constexpr int dummy = 0; (void)dummy;
      const int last = (ks + 6 < 31) ? ks + 6 : 31;
      switch (last - (ks + 1)) {
        case 5: K2_WAIT(20, ks + 1); break;
        case 4: K2_WAIT(16, ks + 1); break;
        case 3: K2_WAIT(12, ks + 1); break;
        case 2: K2_WAIT(8, ks + 1); break;
        case 1: K2_WAIT(4, ks + 1); break;
        default: K2_WAIT(0, ks + 1); break;
      }
    }
    const bf16x8 b0 = __builtin_bit_cast(bf16x8, of[ks & 7][0]), b1 = __builtin_bit_cast(bf16x8, of[ks & 7][1]);
    const bool pre = ks + 7 < 32;
    acc[0][0] = MFMA32(af[ks & 1][0], b0, acc[0][0]);
    SCHED0();
    if (ks < 31) ldfrag(ks + 1);
    if (pre) piece(ks + 7, 0);
    SCHED0();
    acc[0][1] = MFMA32(af[ks & 1][0], b1, acc[0][1]);
    SCHED0();
    if (pre) piece(ks + 7, 1);
    SCHED0();
    acc[1][0] = MFMA32(af[ks & 1][1], b0, acc[1][0]);
    SCHED0();
    if (pre) piece(ks + 7, 2);
    SCHED0();
    acc[1][1] = MFMA32(af[ks & 1][1], b1, acc[1][1]);
    SCHED0();
    if (pre) piece(ks + 7, 3);
    SCHED0();
  }
  STAMP(sb, 2);

  {
    f32x4* rd = (f32x4*)(smem + w * 16384) + lane;
#pragma unroll
    for (int nt = 0; nt < 2; ++nt)
#pragma unroll
      for (int bt = 0; bt < 2; ++bt)
#pragma unroll
        for (int g = 0; g < 4; ++g) {
          f32x4 v = {acc[nt][bt][4 * g], acc[nt][bt][4 * g + 1], acc[nt][bt][4 * g + 2], acc[nt][bt][4 * g + 3]};
          rd[((nt * 2 + bt) * 4 + g) * 64] = v;
        }
  }
  __syncthreads();
  {
    const f32x4* rs = (const f32x4*)smem + (w * 2) * 64 + lane;
    f32x4 s0 = rs[0], s1 = rs[64];
#pragma unroll
    for (int j = 1; j < 8; ++j) {
      s0 += rs[j * 1024];
      s1 += rs[j * 1024 + 64];
    }
    const int tile = w >> 1, nt = tile >> 1, bt = tile & 1, s = w & 1;
    u32x4 o = {cvtpk(s0[0], s0[1]), cvtpk(s0[2], s0[3]), cvtpk(s1[0], s1[1]), cvtpk(s1[2], s1[3])};
    uint16_t* dst = out + (size_t)(rp * 64 + bt * 32 + r) * 1024 + X * 512 + nc * 64 + nt * 32 + s * 16 + hh * 8;
    *(u32x4*)dst = o;
  }
  STAMP(sb, 4);
  STAMP(sb, 5);
}

extern "C" void kernel_launch(void* const* d_in, const int* in_sizes, int n_in, void* d_out, int out_size,
                              void* d_ws, size_t ws_size, hipStream_t stream) {
  (void)in_sizes; (void)n_in; (void)out_size; (void)ws_size;
  (void)hipFuncSetAttribute((const void*)k1_qproj_attn, hipFuncAttributeMaxDynamicSharedMemorySize, K1_LDS);
  (void)hipFuncSetAttribute((const void*)k2_oproj, hipFuncAttributeMaxDynamicSharedMemorySize, K2_LDS);
  const uint16_t* xA = (const uint16_t*)d_in[0];
  const uint16_t* kA = (const uint16_t*)d_in[1];
  const uint16_t* vA = (const uint16_t*)d_in[2];
  const uint16_t* wqA = (const uint16_t*)d_in[3];
  const uint16_t* woA = (const uint16_t*)d_in[4];
  const uint16_t* xB = (const uint16_t*)d_in[5];
  const uint16_t* kB = (const uint16_t*)d_in[6];
  const uint16_t* vB = (const uint16_t*)d_in[7];
  const uint16_t* wqB = (const uint16_t*)d_in[8];
  const uint16_t* woB = (const uint16_t*)d_in[9];
  uint16_t* ows = (uint16_t*)d_ws;
  k1_qproj_attn<<<256, 512, K1_LDS, stream>>>(xA, kA, vA, wqA, xB, kB, vB, wqB, ows, woA, woB);
  k2_oproj<<<256, 512, K2_LDS, stream>>>(woA, woB, ows, (uint16_t*)d_out);
}
```

```cpp
#include <hip/hip_runtime.h>
#include <hip/hip_bf16.h>
#include <stdint.h>

typedef __attribute__((ext_vector_type(8))) __bf16 bf16x8;
typedef __attribute__((ext_vector_type(2))) __bf16 bf16x2;
typedef __attribute__((ext_vector_type(4))) short s16x4;
typedef __attribute__((ext_vector_type(2))) float f32x2;
typedef __attribute__((ext_vector_type(4))) float f32x4;
typedef __attribute__((ext_vector_type(16))) float f32x16;
typedef __attribute__((ext_vector_type(4))) unsigned u32x4;
typedef __attribute__((address_space(3))) s16x4* lds_s16x4_ptr;

#define DI __device__ __forceinline__
#define MFMA32(a, b, c) __builtin_amdgcn_mfma_f32_32x32x16_bf16((a), (b), (c), 0, 0, 0)
#define MEMFENCE() asm volatile("" ::: "memory")
#define SCHED0() __builtin_amdgcn_sched_barrier(0)
#define USE_PRIO 1
#define PRIO(n) __builtin_amdgcn_s_setprio(n)

#define NT_KV 1
#define RDFIRST 1
#define PF_WO 2
#define EARLY_RD true
#define K1_ILV 1
#define PF_WO_ST 7
#define USE_PF 0
#define USE_ROT 0
#define NT_WQ 0
#define NT_X 0
#define NT_WO 0

DI unsigned cvtpk(float lo, float hi) {
  f32x2 v = {lo, hi};
  bf16x2 r = __builtin_convertvector(v, bf16x2);
  return __builtin_bit_cast(unsigned, r);
}
DI bf16x8 pack8(const f32x16& x, int s) {
  u32x4 p = {cvtpk(x[8 * s + 0], x[8 * s + 1]), cvtpk(x[8 * s + 2], x[8 * s + 3]),
             cvtpk(x[8 * s + 4], x[8 * s + 5]), cvtpk(x[8 * s + 6], x[8 * s + 7])};
  return __builtin_bit_cast(bf16x8, p);
}
DI s16x4 tr_read(const char* p) { return __builtin_amdgcn_ds_read_tr16_b64_v4i16((lds_s16x4_ptr)(p)); }
DI bf16x8 tr_frag(const char* p0, const char* p1) {
  s16x4 lo = tr_read(p0), hi = tr_read(p1);
  return __builtin_bit_cast(bf16x8, __builtin_shufflevector(lo, hi, 0, 1, 2, 3, 4, 5, 6, 7));
}
template <int NT>
DI void glds16(const void* gbase, unsigned voff, unsigned ldst) {
  unsigned keep;
  if constexpr (NT)
    asm volatile("s_mov_b32 %0, m0\n\ts_mov_b32 m0, %2\n\ts_nop 0\n\tglobal_load_lds_dwordx4 %1, %3 nt\n\ts_mov_b32 m0, %0"
                 : "=&s"(keep) : "v"(voff), "s"(ldst), "s"(gbase) : "memory");
  else
    asm volatile("s_mov_b32 %0, m0\n\ts_mov_b32 m0, %2\n\ts_nop 0\n\tglobal_load_lds_dwordx4 %1, %3\n\ts_mov_b32 m0, %0"
                 : "=&s"(keep) : "v"(voff), "s"(ldst), "s"(gbase) : "memory");
}
DI void pf4(const void* gbase, unsigned voff, unsigned ldst) {
  unsigned keep;
  asm volatile("s_mov_b32 %0, m0\n\ts_mov_b32 m0, %2\n\ts_nop 0\n\tglobal_load_lds_dword %1, %3\n\ts_mov_b32 m0, %0"
               : "=&s"(keep) : "v"(voff), "s"(ldst), "s"(gbase) : "memory");
}
#define PF_STRIDE 128
DI unsigned lds_addr(const void* p) { return (unsigned)(size_t)(__attribute__((address_space(3))) const char*)p; }
template <int A, int B> DI void wait_vm_sel(int cond) {
  asm volatile("s_waitcnt vmcnt(%2) lgkmcnt(0)\n\ts_cmp_eq_u32 %0, 0\n\ts_cbranch_scc1 .Lw%=\n\ts_waitcnt vmcnt(%1)\n\t.Lw%=:"
               ::"s"(cond), "i"(A), "i"(B) : "memory", "scc");
}
#define STAMP(buf, i)
template <int N> DI void wait_vm() { asm volatile("s_waitcnt vmcnt(%0) lgkmcnt(0)" ::"i"(N) : "memory"); }

constexpr int K1_STAGE = 49152;
constexpr int K1_LDS = 163840;

__global__ __launch_bounds__(512, 2) void k1_qproj_attn(
    const uint16_t* __restrict__ xA, const uint16_t* __restrict__ kA, const uint16_t* __restrict__ vA,
    const uint16_t* __restrict__ wqA, const uint16_t* __restrict__ xB, const uint16_t* __restrict__ kB,
    const uint16_t* __restrict__ vB, const uint16_t* __restrict__ wqB, uint16_t* __restrict__ ows,
    const uint16_t* __restrict__ woA, const uint16_t* __restrict__ woB) {
  extern __shared__ __attribute__((aligned(16))) char smem[];
  const int tid = threadIdx.x, lane = tid & 63;
  const int w = __builtin_amdgcn_readfirstlane(tid >> 6);
  const int bid = blockIdx.x, xcd = bid & 7, idx = bid >> 3;
  const int X = xcd >> 2, h = (xcd & 3) * 8 + (idx & 7), bg = idx >> 3;
  const int c = w & 3;
  unsigned long long* sb = (unsigned long long*)((char*)ows + (64u << 20)); (void)sb;
  STAMP(sb, 0);
  const int r = lane & 31, hh = lane >> 5, blk = (lane >> 4) & 1, q = (lane & 15) >> 2, p = lane & 3;
  const int troff = (8 * hh + q) * 256 + 32 * blk + 16 * (p & 1) + 8 * (p >> 1);
  int tro[4];
#pragma unroll
  for (int t = 0; t < 4; ++t) tro[t] = troff + ((t ^ q) << 6);
  bf16x8 qf[8];

  if (w < 4) {
    f32x16 acc[4][2];
#pragma unroll
    for (int t = 0; t < 4; ++t)
#pragma unroll
      for (int u = 0; u < 2; ++u)
#pragma unroll
        for (int i = 0; i < 16; ++i) acc[t][u][i] = 0.f;
    const int f = (r >> 1) & 7;
    int xo[4];
#pragma unroll
    for (int s = 0; s < 4; ++s) xo[s] = 16384 + c * 8192 + r * 128 + (((2 * s + hh) ^ f) << 4);
    bf16x8 af[2][4], bf[2][2];
    auto rdA = [&](const char* lb, int s, int slot, int t) {
      af[slot][t] = tr_frag(lb + tro[t] + s * 4096, lb + tro[t] + s * 4096 + 1024);
    };
    auto rdB = [&](const char* lb, int s, int slot) {
      bf[slot][0] = *(const bf16x8*)(lb + xo[s]);
      bf[slot][1] = *(const bf16x8*)(lb + xo[s] + 4096);
    };
    auto kstep = [&](int slot, const char* lb, int s) {
      const int ns = slot ^ 1;
      acc[0][0] = MFMA32(af[slot][0], bf[slot][0], acc[0][0]); SCHED0();
      rdB(lb, s, ns); SCHED0();
      acc[0][1] = MFMA32(af[slot][0], bf[slot][1], acc[0][1]); SCHED0();
      rdA(lb, s, ns, 0); SCHED0();
      acc[1][0] = MFMA32(af[slot][1], bf[slot][0], acc[1][0]); SCHED0();
      rdA(lb, s, ns, 1); SCHED0();
      acc[1][1] = MFMA32(af[slot][1], bf[slot][1], acc[1][1]); SCHED0();
      rdA(lb, s, ns, 2); SCHED0();
      acc[2][0] = MFMA32(af[slot][2], bf[slot][0], acc[2][0]); SCHED0();
      rdA(lb, s, ns, 3); SCHED0();
      acc[2][1] = MFMA32(af[slot][2], bf[slot][1], acc[2][1]);
      acc[3][0] = MFMA32(af[slot][3], bf[slot][0], acc[3][0]);
      acc[3][1] = MFMA32(af[slot][3], bf[slot][1], acc[3][1]); SCHED0();
    };
    __builtin_amdgcn_s_barrier();
    MEMFENCE();
    STAMP(sb, 1);
    rdB(smem, 0, 0);
#pragma unroll
    for (int t = 0; t < 4; ++t) rdA(smem, 0, 0, t);
    SCHED0();
    unsigned cur = 0;
#pragma unroll 1
    for (int st = 0; st < 8; ++st) {
      const unsigned nxt = (cur == 2 * K1_STAGE) ? 0u : cur + K1_STAGE;
      const char* lb = smem + cur;
      kstep(0, lb, 1);
      kstep(1, lb, 2);
      kstep(0, lb, 3);
      asm volatile("s_waitcnt lgkmcnt(0)" ::: "memory");
      __builtin_amdgcn_s_barrier();
      MEMFENCE();
      kstep(1, smem + nxt, 0);
      cur = nxt;
    }
    STAMP(sb, 2);
    u32x4* qd = (u32x4*)(smem + 131072 + c * 8192) + lane;
#pragma unroll
    for (int t = 0; t < 4; ++t)
#pragma unroll
      for (int s = 0; s < 2; ++s) {
        qd[(2 * t + s) * 64] = __builtin_bit_cast(u32x4, pack8(acc[t][1], s));
        qf[2 * t + s] = pack8(acc[t][0], s);
      }
    asm volatile("s_waitcnt lgkmcnt(0)" ::: "memory");
    __builtin_amdgcn_s_barrier();
    MEMFENCE();
  } else {
    const uint16_t* x = X ? xB : xA;
    const uint16_t* kc = X ? kB : kA;
    const uint16_t* vc = X ? vB : vA;
    const uint16_t* wq = X ? wqB : wqA;
    const char* xb = (const char*)(x + (size_t)((bg * 8 + c * 2) * 32) * 512);
    const char* wb = (const char*)(wq + (size_t)(c * 16) * 4096 + h * 128);
    const unsigned xoff0 = (lane >> 3) * 1024 + (((lane & 7) ^ (lane >> 4)) << 4);
    const unsigned xoff1 = (8 + (lane >> 3)) * 1024 + (((lane & 7) ^ ((lane >> 4) | 4)) << 4);
    const unsigned woff = (lane >> 4) * 8192 + (((lane & 15) ^ ((lane >> 4) << 2)) << 4);
    const unsigned sbase = lds_addr(smem);
    auto stage = [&](int st, unsigned bufoff) {
      const unsigned lb = sbase + bufoff;
      const char* wg = wb + (size_t)st * (64 * 8192);
#pragma unroll
      for (int j = 0; j < 4; ++j) glds16<NT_WQ>(wg + j * (4 * 8192), woff, lb + c * 4096 + j * 1024);
      const unsigned lx = lb + 16384 + c * 8192;
      const char* xg = xb + st * 128;
#pragma unroll
      for (int j = 0; j < 4; ++j) {
        glds16<NT_X>(xg + j * 16384, xoff0, lx + j * 2048);
        glds16<NT_X>(xg + j * 16384, xoff1, lx + j * 2048 + 1024);
      }
    };
    const unsigned kvo0 = lane * 16, kvo1 = lane * 16 + 4096;
    u32x4 kv[2][16];
#pragma unroll
    for (int u = 0; u < 2; ++u)
#pragma unroll
      for (int i = 0; i < 16; ++i) kv[u][i] = u32x4{0, 0, 0, 0};
#define KVNT " nt"
    auto kvload_if = [&](int cond_) {
      const int cond = __builtin_amdgcn_readfirstlane(cond_);
#pragma unroll
      for (int u = 0; u < 2; ++u) {
        const size_t kvbase = (size_t)((bg * 8 + c * 2 + u) * 32 + h) * 4096;
        const uint16_t* kb = kc + kvbase;
        const uint16_t* vb = vc + kvbase;
        asm volatile(
            "s_cmp_eq_u32 %16, 0\n\t"
            "s_cbranch_scc1 .Lkv%=\n\t"
            "global_load_dwordx4 %0, %17, %19" KVNT "\n\t"
            "global_load_dwordx4 %1, %17, %19 offset:1024" KVNT "\n\t"
            "global_load_dwordx4 %2, %17, %19 offset:2048" KVNT "\n\t"
            "global_load_dwordx4 %3, %17, %19 offset:3072" KVNT "\n\t"
            "global_load_dwordx4 %4, %18, %19" KVNT "\n\t"
            "global_load_dwordx4 %5, %18, %19 offset:1024" KVNT "\n\t"
            "global_load_dwordx4 %6, %18, %19 offset:2048" KVNT "\n\t"
            "global_load_dwordx4 %7, %18, %19 offset:3072" KVNT "\n\t"
            "global_load_dwordx4 %8, %17, %20" KVNT "\n\t"
            "global_load_dwordx4 %9, %17, %20 offset:1024" KVNT "\n\t"
            "global_load_dwordx4 %10, %17, %20 offset:2048" KVNT "\n\t"
            "global_load_dwordx4 %11, %17, %20 offset:3072" KVNT "\n\t"
            "global_load_dwordx4 %12, %18, %20" KVNT "\n\t"
            "global_load_dwordx4 %13, %18, %20 offset:1024" KVNT "\n\t"
            "global_load_dwordx4 %14, %18, %20 offset:2048" KVNT "\n\t"
            "global_load_dwordx4 %15, %18, %20 offset:3072" KVNT "\n\t"
            ".Lkv%=:"
            : "+v"(kv[u][0]), "+v"(kv[u][1]), "+v"(kv[u][2]), "+v"(kv[u][3]), "+v"(kv[u][4]), "+v"(kv[u][5]), "+v"(kv[u][6]), "+v"(kv[u][7]),
              "+v"(kv[u][8]), "+v"(kv[u][9]), "+v"(kv[u][10]), "+v"(kv[u][11]), "+v"(kv[u][12]), "+v"(kv[u][13]), "+v"(kv[u][14]), "+v"(kv[u][15])
            : "s"(cond), "v"(kvo0), "v"(kvo1), "s"(kb), "s"(vb)
            : "memory", "scc");
      }
    };
#define K1_GRP ((idx >> 0) & 1)
    const int early = __builtin_amdgcn_readfirstlane(K1_GRP);
    kvload_if(early);
    stage(0, 0);
    stage(1, K1_STAGE);
    stage(2, 2 * K1_STAGE);
    asm volatile("s_waitcnt vmcnt(24)" ::: "memory");
    __builtin_amdgcn_s_barrier();
    MEMFENCE();
    unsigned cur = 0;
#pragma unroll 1
    for (int st = 0; st < 8; ++st) {
      asm volatile(
          "s_waitcnt vmcnt(44)\n\t"
          "s_cmp_eq_u32 %0, 5\n\t"
          "s_cbranch_scc1 .Lw%=\n\t"
          "s_waitcnt vmcnt(32)\n\t"
          "s_cmp_ge_u32 %0, 6\n\t"
          "s_cbranch_scc1 .Lw%=\n\t"
          "s_waitcnt vmcnt(12)\n\t"
          "s_cmp_lt_u32 %1, 6\n\t"
          "s_cbranch_scc1 .Lw%=\n\t"
          "s_waitcnt vmcnt(0)\n\t"
          ".Lw%=:" ::"s"(early ? 0 : st), "s"(st) : "memory", "scc");
      __builtin_amdgcn_s_barrier();
      MEMFENCE();
      if (st < 5) stage(st + 3, cur);
      kvload_if(st == 4 && !early);
      cur = (cur == 2 * K1_STAGE) ? 0u : cur + K1_STAGE;
    }
#pragma unroll
    for (int u = 0; u < 2; ++u) {
      asm volatile("s_waitcnt vmcnt(%16)"
                   : "+v"(kv[u][0]), "+v"(kv[u][1]), "+v"(kv[u][2]), "+v"(kv[u][3]), "+v"(kv[u][4]), "+v"(kv[u][5]), "+v"(kv[u][6]), "+v"(kv[u][7]),
                     "+v"(kv[u][8]), "+v"(kv[u][9]), "+v"(kv[u][10]), "+v"(kv[u][11]), "+v"(kv[u][12]), "+v"(kv[u][13]), "+v"(kv[u][14]), "+v"(kv[u][15])
                   : "i"(u == 0 ? 16 : 0) : "memory");
      char* kl = smem + (c * 2 + u) * 8192 + (lane >> 4) * 256;
#pragma unroll
      for (int i = 0; i < 8; ++i) {
        *(u32x4*)(kl + i * 1024 + (((lane & 15) ^ ((4 * i + (lane >> 4)) & 15)) << 4)) = kv[u][i];
        *(u32x4*)(kl + 65536 + i * 1024 + (((lane & 15) ^ ((lane >> 4) << 2)) << 4)) = kv[u][8 + i];
      }
    }
    asm volatile("s_waitcnt lgkmcnt(0)" ::: "memory");
    __builtin_amdgcn_s_barrier();
    MEMFENCE();
    const u32x4* qs = (const u32x4*)(smem + 131072 + c * 8192) + lane;
#pragma unroll
    for (int i = 0; i < 8; ++i) qf[i] = __builtin_bit_cast(bf16x8, qs[i * 64]);
  }

  const int unit = c * 2 + (w >> 2), b = bg * 8 + unit;
  unsigned pfd;
  {
    const char* pw = (const char*)((bid & 1) ? woB : woA) + (size_t)(bid >> 1) * 32768 + w * 4096;
    asm volatile("global_load_dword %0, %1, %2" : "=v"(pfd) : "v"(lane * 64), "s"(pw) : "memory");
  }
  const char* kl = smem + unit * 8192;
  const char* vl = kl + 65536;
  const int pr = (r & 0x13) | ((r & 4) << 1) | ((r & 8) >> 1);
  f32x16 sacc, sac1;
#pragma unroll
  for (int i = 0; i < 16; ++i) { sacc[i] = 0.f; sac1[i] = 0.f; }
#pragma unroll
  for (int i = 0; i < 8; i += 2) {
    sacc = MFMA32(*(const bf16x8*)(kl + pr * 256 + (((2 * i + hh) ^ (pr & 15)) << 4)), qf[i], sacc);
    sac1 = MFMA32(*(const bf16x8*)(kl + pr * 256 + (((2 * i + 2 + hh) ^ (pr & 15)) << 4)), qf[i + 1], sac1);
  }
  sacc += sac1;
  STAMP(sb, 3);

  const float scale = 0.08838834764831845f;
  float m = -3.0e38f;
#pragma unroll
  for (int i = 0; i < 16; i += 2) {
    unsigned u = cvtpk(sacc[i], sacc[i + 1]);
    float a = __uint_as_float(u << 16) * scale, cc = __uint_as_float(u & 0xffff0000u) * scale;
    unsigned u2 = cvtpk(a, cc);
    sacc[i] = __uint_as_float(u2 << 16);
    sacc[i + 1] = __uint_as_float(u2 & 0xffff0000u);
    m = fmaxf(m, fmaxf(sacc[i], sacc[i + 1]));
  }
  {
    auto rr = __builtin_amdgcn_permlane32_swap(__float_as_uint(m), __float_as_uint(m), false, false);
    m = fmaxf(__uint_as_float(rr[0]), __uint_as_float(rr[1]));
  }
  float sum = 0.f;
#pragma unroll
  for (int i = 0; i < 16; ++i) {
    sacc[i] = __builtin_amdgcn_exp2f((sacc[i] - m) * 1.4426950408889634f);
    sum += sacc[i];
  }
  {
    auto rr = __builtin_amdgcn_permlane32_swap(__float_as_uint(sum), __float_as_uint(sum), false, false);
    sum = __uint_as_float(rr[0]) + __uint_as_float(rr[1]);
  }
  const float inv = 1.0f / sum;
#pragma unroll
  for (int i = 0; i < 16; ++i) sacc[i] *= inv;
  bf16x8 pf0 = pack8(sacc, 0), pf1 = pack8(sacc, 1);

  u32x4* od = (u32x4*)(ows + ((size_t)((X * 32 + b) * 256 + h * 8) * 64 + lane) * 8);
#pragma unroll
  for (int t = 0; t < 4; ++t) {
    f32x16 o;
#pragma unroll
    for (int i = 0; i < 16; ++i) o[i] = 0.f;
    const char* pa = vl + tro[t];
    o = MFMA32(tr_frag(pa, pa + 1024), pf0, o);
    o = MFMA32(tr_frag(pa + 4096, pa + 4096 + 1024), pf1, o);
    od[(2 * t) * 64] = __builtin_bit_cast(u32x4, pack8(o, 0));
    od[(2 * t + 1) * 64] = __builtin_bit_cast(u32x4, pack8(o, 1));
  }
  STAMP(sb, 4);
  asm volatile("s_waitcnt vmcnt(0)" : "+v"(pfd)::"memory");
  STAMP(sb, 5);
}

constexpr int K2_LDS = 131072;

__global__ __launch_bounds__(512, 2) void k2_oproj(const uint16_t* __restrict__ woA, const uint16_t* __restrict__ woB,
                                                   const uint16_t* __restrict__ ows, uint16_t* __restrict__ out) {
  extern __shared__ __attribute__((aligned(16))) char smem[];
  const int tid = threadIdx.x, lane = tid & 63;
  const int w = __builtin_amdgcn_readfirstlane(tid >> 6);
  const int bid = blockIdx.x, xcd = bid & 7, idx = bid >> 3;
  const int X = xcd >> 2, rp = (xcd & 3) * 4 + (idx >> 3), nc = idx & 7;
  const uint16_t* wo = X ? woB : woA;
  unsigned long long* sb = (unsigned long long*)((char*)ows + (65u << 20)); (void)sb;
  STAMP(sb, 0);
  const int r = lane & 31, hh = lane >> 5, blk = (lane >> 4) & 1, q = (lane & 15) >> 2, p = lane & 3;

  const char* wb = (const char*)(wo + (size_t)(w * 512) * 512 + nc * 64);
  const unsigned woff = (lane >> 3) * 1024 + (((lane & 7) ^ (((lane >> 4) & 1) << 2)) << 4);
  const char* lw = smem + w * 16384;
  const unsigned lwa = lds_addr(smem) + w * 16384;
  const char* ob = (const char*)ows + (size_t)((X * 32 + rp * 2) * 256 + w * 32) * 1024;
  const unsigned oo0 = lane * 16, oo1 = lane * 16 + 262144;

  u32x4 of[8][2];
  f32x16 acc[2][2];
#pragma unroll
  for (int a = 0; a < 2; ++a)
#pragma unroll
    for (int c = 0; c < 2; ++c)
#pragma unroll
      for (int i = 0; i < 16; ++i) acc[a][c][i] = 0.f;

#define K2_ROT 0
  const int rot = K2_ROT;
  auto piece = [&](int ks_, int j) {
    const int slot = ks_ & 7, ks = (ks_ + rot) & 31;
    if (j == 0) glds16<NT_WO>(wb + (size_t)ks * 16384, woff, lwa + slot * 2048);
    if (j == 1) glds16<NT_WO>(wb + (size_t)ks * 16384 + 8192, woff, lwa + slot * 2048 + 1024);
    if (j == 2) asm volatile("global_load_dwordx4 %0, %1, %2" : "=v"(of[slot][0]) : "v"(oo0), "s"(ob + ks * 1024) : "memory");
    if (j == 3) asm volatile("global_load_dwordx4 %0, %1, %2" : "=v"(of[slot][1]) : "v"(oo1), "s"(ob + ks * 1024) : "memory");
  };

  const int troff = (8 * hh + q) * 128 + 32 * blk + 16 * (p & 1) + 8 * (p >> 1);
  const int hsw = (q >> 1) & 1;
  const char* pa0 = lw + troff + ((0 ^ hsw) << 6);
  const char* pa1 = lw + troff + ((1 ^ hsw) << 6);
  bf16x8 af[2][2];
  auto ldfrag = [&](int ks) {
    const int o = (ks & 7) * 2048;
    af[ks & 1][0] = tr_frag(pa0 + o, pa0 + o + 512);
    af[ks & 1][1] = tr_frag(pa1 + o, pa1 + o + 512);
  };
#define K2_WAIT(N, ks) asm volatile("s_waitcnt vmcnt(%2)" : "+v"(of[(ks) & 7][0]), "+v"(of[(ks) & 7][1]) : "i"(N) : "memory")

#pragma unroll
  for (int ks = 0; ks < 7; ++ks)
#pragma unroll
    for (int j = 0; j < 4; ++j) piece(ks, j);
  K2_WAIT(24, 0);
  STAMP(sb, 1);
  ldfrag(0);

#pragma unroll
  for (int ks = 0; ks < 32; ++ks) {
    if (ks < 31) {
      constexpr int dummy = 0; (void)dummy;
      const int last = (ks + 6 < 31) ? ks + 6 : 31;
      switch (last - (ks + 1)) {
        case 5: K2_WAIT(20, ks + 1); break;
        case 4: K2_WAIT(16, ks + 1); break;
        case 3: K2_WAIT(12, ks + 1); break;
        case 2: K2_WAIT(8, ks + 1); break;
        case 1: K2_WAIT(4, ks + 1); break;
        default: K2_WAIT(0, ks + 1); break;
      }
    }
    const bf16x8 b0 = __builtin_bit_cast(bf16x8, of[ks & 7][0]), b1 = __builtin_bit_cast(bf16x8, of[ks & 7][1]);
    const bool pre = ks + 7 < 32;
    acc[0][0] = MFMA32(af[ks & 1][0], b0, acc[0][0]);
    SCHED0();
    if (ks < 31) ldfrag(ks + 1);
    if (pre) piece(ks + 7, 0);
    SCHED0();
    acc[0][1] = MFMA32(af[ks & 1][0], b1, acc[0][1]);
    SCHED0();
    if (pre) piece(ks + 7, 1);
    SCHED0();
    acc[1][0] = MFMA32(af[ks & 1][1], b0, acc[1][0]);
    SCHED0();
    if (pre) piece(ks + 7, 2);
    SCHED0();
    acc[1][1] = MFMA32(af[ks & 1][1], b1, acc[1][1]);
    SCHED0();
    if (pre) piece(ks + 7, 3);
    SCHED0();
  }
  STAMP(sb, 2);

  {
    f32x4* rd = (f32x4*)(smem + w * 16384) + lane;
#pragma unroll
    for (int nt = 0; nt < 2; ++nt)
#pragma unroll
      for (int bt = 0; bt < 2; ++bt)
#pragma unroll
        for (int g = 0; g < 4; ++g) {
          f32x4 v = {acc[nt][bt][4 * g], acc[nt][bt][4 * g + 1], acc[nt][bt][4 * g + 2], acc[nt][bt][4 * g + 3]};
          rd[((nt * 2 + bt) * 4 + g) * 64] = v;
        }
  }
  __syncthreads();
  {
    const f32x4* rs = (const f32x4*)smem + (w * 2) * 64 + lane;
    f32x4 s0 = rs[0], s1 = rs[64];
#pragma unroll
    for (int j = 1; j < 8; ++j) {
      s0 += rs[j * 1024];
      s1 += rs[j * 1024 + 64];
    }
    const int tile = w >> 1, nt = tile >> 1, bt = tile & 1, s = w & 1;
    u32x4 o = {cvtpk(s0[0], s0[1]), cvtpk(s0[2], s0[3]), cvtpk(s1[0], s1[1]), cvtpk(s1[2], s1[3])};
    uint16_t* dst = out + (size_t)(rp * 64 + bt * 32 + r) * 1024 + X * 512 + nc * 64 + nt * 32 + s * 16 + hh * 8;
    *(u32x4*)dst = o;
  }
  STAMP(sb, 4);
  STAMP(sb, 5);
}

extern "C" void kernel_launch(void* const* d_in, const int* in_sizes, int n_in, void* d_out, int out_size,
                              void* d_ws, size_t ws_size, hipStream_t stream) {
  (void)in_sizes; (void)n_in; (void)out_size; (void)ws_size;
  (void)hipFuncSetAttribute((const void*)k1_qproj_attn, hipFuncAttributeMaxDynamicSharedMemorySize, K1_LDS);
  (void)hipFuncSetAttribute((const void*)k2_oproj, hipFuncAttributeMaxDynamicSharedMemorySize, K2_LDS);
  const uint16_t* xA = (const uint16_t*)d_in[0];
  const uint16_t* kA = (const uint16_t*)d_in[1];
  const uint16_t* vA = (const uint16_t*)d_in[2];
  const uint16_t* wqA = (const uint16_t*)d_in[3];
  const uint16_t* woA = (const uint16_t*)d_in[4];
  const uint16_t* xB = (const uint16_t*)d_in[5];
  const uint16_t* kB = (const uint16_t*)d_in[6];
  const uint16_t* vB = (const uint16_t*)d_in[7];
  const uint16_t* wqB = (const uint16_t*)d_in[8];
  const uint16_t* woB = (const uint16_t*)d_in[9];
  uint16_t* ows = (uint16_t*)d_ws;
  k1_qproj_attn<<<256, 512, K1_LDS, stream>>>(xA, kA, vA, wqA, xB, kB, vB, wqB, ows, woA, woB);
  k2_oproj<<<256, 512, K2_LDS, stream>>>(woA, woB, ows, (uint16_t*)d_out);
}
```

```cpp
#include <hip/hip_runtime.h>
#include <hip/hip_bf16.h>
#include <stdint.h>

typedef __attribute__((ext_vector_type(8))) __bf16 bf16x8;
typedef __attribute__((ext_vector_type(2))) __bf16 bf16x2;
typedef __attribute__((ext_vector_type(4))) short s16x4;
typedef __attribute__((ext_vector_type(2))) float f32x2;
typedef __attribute__((ext_vector_type(4))) float f32x4;
typedef __attribute__((ext_vector_type(16))) float f32x16;
typedef __attribute__((ext_vector_type(4))) unsigned u32x4;
typedef __attribute__((address_space(3))) s16x4* lds_s16x4_ptr;

#define DI __device__ __forceinline__
#define MFMA32(a, b, c) __builtin_amdgcn_mfma_f32_32x32x16_bf16((a), (b), (c), 0, 0, 0)
#define MEMFENCE() asm volatile("" ::: "memory")
#define SCHED0() __builtin_amdgcn_sched_barrier(0)
#define USE_PRIO 1
#define PRIO(n) __builtin_amdgcn_s_setprio(n)

#define NT_KV 1
#define RDFIRST 1
#define OST 0
#define KVFRONT 0
#define PF_WO 2
#define EARLY_RD true
#define K1_ILV 1
#define PF_WO_ST 7
#define USE_PF 0
#define USE_ROT 0
#define NT_WQ 1
#define NT_X 0
#define NT_WO 0

DI unsigned cvtpk(float lo, float hi) {
  f32x2 v = {lo, hi};
  bf16x2 r = __builtin_convertvector(v, bf16x2);
  return __builtin_bit_cast(unsigned, r);
}
DI bf16x8 pack8(const f32x16& x, int s) {
  u32x4 p = {cvtpk(x[8 * s + 0], x[8 * s + 1]), cvtpk(x[8 * s + 2], x[8 * s + 3]),
             cvtpk(x[8 * s + 4], x[8 * s + 5]), cvtpk(x[8 * s + 6], x[8 * s + 7])};
  return __builtin_bit_cast(bf16x8, p);
}
DI s16x4 tr_read(const char* p) { return __builtin_amdgcn_ds_read_tr16_b64_v4i16((lds_s16x4_ptr)(p)); }
DI bf16x8 tr_frag(const char* p0, const char* p1) {
  s16x4 lo = tr_read(p0), hi = tr_read(p1);
  return __builtin_bit_cast(bf16x8, __builtin_shufflevector(lo, hi, 0, 1, 2, 3, 4, 5, 6, 7));
}
template <int NT>
DI void glds16(const void* gbase, unsigned voff, unsigned ldst) {
  unsigned keep;
  if constexpr (NT)
    asm volatile("s_mov_b32 %0, m0\n\ts_mov_b32 m0, %2\n\ts_nop 0\n\tglobal_load_lds_dwordx4 %1, %3 nt\n\ts_mov_b32 m0, %0"
                 : "=&s"(keep) : "v"(voff), "s"(ldst), "s"(gbase) : "memory");
  else
    asm volatile("s_mov_b32 %0, m0\n\ts_mov_b32 m0, %2\n\ts_nop 0\n\tglobal_load_lds_dwordx4 %1, %3\n\ts_mov_b32 m0, %0"
                 : "=&s"(keep) : "v"(voff), "s"(ldst), "s"(gbase) : "memory");
}
DI void pf4(const void* gbase, unsigned voff, unsigned ldst) {
  unsigned keep;
  asm volatile("s_mov_b32 %0, m0\n\ts_mov_b32 m0, %2\n\ts_nop 0\n\tglobal_load_lds_dword %1, %3\n\ts_mov_b32 m0, %0"
               : "=&s"(keep) : "v"(voff), "s"(ldst), "s"(gbase) : "memory");
}
#define PF_STRIDE 128
DI unsigned lds_addr(const void* p) { return (unsigned)(size_t)(__attribute__((address_space(3))) const char*)p; }
template <int A, int B> DI void wait_vm_sel(int cond) {
  asm volatile("s_waitcnt vmcnt(%2) lgkmcnt(0)\n\ts_cmp_eq_u32 %0, 0\n\ts_cbranch_scc1 .Lw%=\n\ts_waitcnt vmcnt(%1)\n\t.Lw%=:"
               ::"s"(cond), "i"(A), "i"(B) : "memory", "scc");
}
#define STAMP(buf, i)
template <int N> DI void wait_vm() { asm volatile("s_waitcnt vmcnt(%0) lgkmcnt(0)" ::"i"(N) : "memory"); }

constexpr int K1_STAGE = 49152;
constexpr int K1_LDS = 163840;

__global__ __launch_bounds__(512, 2) void k1_qproj_attn(
    const uint16_t* __restrict__ xA, const uint16_t* __restrict__ kA, const uint16_t* __restrict__ vA,
    const uint16_t* __restrict__ wqA, const uint16_t* __restrict__ xB, const uint16_t* __restrict__ kB,
    const uint16_t* __restrict__ vB, const uint16_t* __restrict__ wqB, uint16_t* __restrict__ ows,
    const uint16_t* __restrict__ woA, const uint16_t* __restrict__ woB) {
  extern __shared__ __attribute__((aligned(16))) char smem[];
  const int tid = threadIdx.x, lane = tid & 63;
  const int w = __builtin_amdgcn_readfirstlane(tid >> 6);
  const int bid = blockIdx.x, xcd = bid & 7, idx = bid >> 3;
  const int X = xcd >> 2, h = (xcd & 3) * 8 + (idx & 7), bg = idx >> 3;
  const int c = w & 3;
  unsigned long long* sb = (unsigned long long*)((char*)ows + (64u << 20)); (void)sb;
  STAMP(sb, 0);
  const int r = lane & 31, hh = lane >> 5, blk = (lane >> 4) & 1, q = (lane & 15) >> 2, p = lane & 3;
  const int troff = (8 * hh + q) * 256 + 32 * blk + 16 * (p & 1) + 8 * (p >> 1);
  int tro[4];
#pragma unroll
  for (int t = 0; t < 4; ++t) tro[t] = troff + ((t ^ q) << 6);
  bf16x8 qf[8];

  if (w < 4) {
    f32x16 acc[4][2];
#pragma unroll
    for (int t = 0; t < 4; ++t)
#pragma unroll
      for (int u = 0; u < 2; ++u)
#pragma unroll
        for (int i = 0; i < 16; ++i) acc[t][u][i] = 0.f;
    const int f = (r >> 1) & 7;
    int xo[4];
#pragma unroll
    for (int s = 0; s < 4; ++s) xo[s] = 16384 + c * 8192 + r * 128 + (((2 * s + hh) ^ f) << 4);
    bf16x8 af[2][4], bf[2][2];
    auto rdA = [&](const char* lb, int s, int slot, int t) {
      af[slot][t] = tr_frag(lb + tro[t] + s * 4096, lb + tro[t] + s * 4096 + 1024);
    };
    auto rdB = [&](const char* lb, int s, int slot) {
      bf[slot][0] = *(const bf16x8*)(lb + xo[s]);
      bf[slot][1] = *(const bf16x8*)(lb + xo[s] + 4096);
    };
    auto kstep = [&](int slot, const char* lb, int s) {
      const int ns = slot ^ 1;
      acc[0][0] = MFMA32(af[slot][0], bf[slot][0], acc[0][0]); SCHED0();
      rdB(lb, s, ns); SCHED0();
      acc[0][1] = MFMA32(af[slot][0], bf[slot][1], acc[0][1]); SCHED0();
      rdA(lb, s, ns, 0); SCHED0();
      acc[1][0] = MFMA32(af[slot][1], bf[slot][0], acc[1][0]); SCHED0();
      rdA(lb, s, ns, 1); SCHED0();
      acc[1][1] = MFMA32(af[slot][1], bf[slot][1], acc[1][1]); SCHED0();
      rdA(lb, s, ns, 2); SCHED0();
      acc[2][0] = MFMA32(af[slot][2], bf[slot][0], acc[2][0]); SCHED0();
      rdA(lb, s, ns, 3); SCHED0();
      acc[2][1] = MFMA32(af[slot][2], bf[slot][1], acc[2][1]);
      acc[3][0] = MFMA32(af[slot][3], bf[slot][0], acc[3][0]);
      acc[3][1] = MFMA32(af[slot][3], bf[slot][1], acc[3][1]); SCHED0();
    };
    __builtin_amdgcn_s_barrier();
    MEMFENCE();
    STAMP(sb, 1);
    rdB(smem, 0, 0);
#pragma unroll
    for (int t = 0; t < 4; ++t) rdA(smem, 0, 0, t);
    SCHED0();
    unsigned cur = 0;
#pragma unroll 1
    for (int st = 0; st < 8; ++st) {
      const unsigned nxt = (cur == 2 * K1_STAGE) ? 0u : cur + K1_STAGE;
      const char* lb = smem + cur;
      kstep(0, lb, 1);
      kstep(1, lb, 2);
      kstep(0, lb, 3);
      asm volatile("s_waitcnt lgkmcnt(0)" ::: "memory");
      __builtin_amdgcn_s_barrier();
      MEMFENCE();
      kstep(1, smem + nxt, 0);
      cur = nxt;
    }
    STAMP(sb, 2);
    u32x4* qd = (u32x4*)(smem + 131072 + c * 8192) + lane;
#pragma unroll
    for (int t = 0; t < 4; ++t)
#pragma unroll
      for (int s = 0; s < 2; ++s) {
        qd[(2 * t + s) * 64] = __builtin_bit_cast(u32x4, pack8(acc[t][1], s));
        qf[2 * t + s] = pack8(acc[t][0], s);
      }
    asm volatile("s_waitcnt lgkmcnt(0)" ::: "memory");
    __builtin_amdgcn_s_barrier();
    MEMFENCE();
  } else {
    const uint16_t* x = X ? xB : xA;
    const uint16_t* kc = X ? kB : kA;
    const uint16_t* vc = X ? vB : vA;
    const uint16_t* wq = X ? wqB : wqA;
    const char* xb = (const char*)(x + (size_t)((bg * 8 + c * 2) * 32) * 512);
    const char* wb = (const char*)(wq + (size_t)(c * 16) * 4096 + h * 128);
    const unsigned xoff0 = (lane >> 3) * 1024 + (((lane & 7) ^ (lane >> 4)) << 4);
    const unsigned xoff1 = (8 + (lane >> 3)) * 1024 + (((lane & 7) ^ ((lane >> 4) | 4)) << 4);
    const unsigned woff = (lane >> 4) * 8192 + (((lane & 15) ^ ((lane >> 4) << 2)) << 4);
    const unsigned sbase = lds_addr(smem);
    auto stage = [&](int st, unsigned bufoff) {
      const unsigned lb = sbase + bufoff;
      const char* wg = wb + (size_t)st * (64 * 8192);
#pragma unroll
      for (int j = 0; j < 4; ++j) glds16<NT_WQ>(wg + j * (4 * 8192), woff, lb + c * 4096 + j * 1024);
      const unsigned lx = lb + 16384 + c * 8192;
      const char* xg = xb + st * 128;
#pragma unroll
      for (int j = 0; j < 4; ++j) {
        glds16<NT_X>(xg + j * 16384, xoff0, lx + j * 2048);
        glds16<NT_X>(xg + j * 16384, xoff1, lx + j * 2048 + 1024);
      }
    };
    const unsigned kvo = lane * 16;
    u32x4 kv[2][16];
#define KVNT " nt"
    auto kvchunk = [&](int ch) {
      const int u = ch >> 2, isv = (ch >> 1) & 1, o = (ch & 3) * 4;
      const uint16_t* g = (isv ? vc : kc) + (size_t)((bg * 8 + c * 2 + u) * 32 + h) * 4096 + (ch & 1) * 2048;
      asm volatile(
          "global_load_dwordx4 %0, %4, %5" KVNT "\n\t"
          "global_load_dwordx4 %1, %4, %5 offset:1024" KVNT "\n\t"
          "global_load_dwordx4 %2, %4, %5 offset:2048" KVNT "\n\t"
          "global_load_dwordx4 %3, %4, %5 offset:3072" KVNT
          : "=v"(kv[u][o]), "=v"(kv[u][o + 1]), "=v"(kv[u][o + 2]), "=v"(kv[u][o + 3])
          : "v"(kvo), "s"(g) : "memory");
    };
    stage(0, 0);
    stage(1, K1_STAGE);
    stage(2, 2 * K1_STAGE);
    kvchunk(0);
    kvchunk(1);
    asm volatile("s_waitcnt vmcnt(32)" ::: "memory");
    __builtin_amdgcn_s_barrier();
    MEMFENCE();
#pragma unroll
    for (int st = 0; st < 8; ++st) {
      if (st == 1) asm volatile("s_waitcnt vmcnt(24)" ::: "memory");
      else if (st < 6) asm volatile("s_waitcnt vmcnt(20)" ::: "memory");
      else if (st == 6) asm volatile("s_waitcnt vmcnt(8)" ::: "memory");
      __builtin_amdgcn_s_barrier();
      MEMFENCE();
      if (st < 5) stage(st + 3, (st % 3) * K1_STAGE);
      if (st < 6) kvchunk(st + 2);
    }
#pragma unroll
    for (int u = 0; u < 2; ++u) {
      asm volatile("s_waitcnt vmcnt(%16)"
                   : "+v"(kv[u][0]), "+v"(kv[u][1]), "+v"(kv[u][2]), "+v"(kv[u][3]), "+v"(kv[u][4]), "+v"(kv[u][5]), "+v"(kv[u][6]), "+v"(kv[u][7]),
                     "+v"(kv[u][8]), "+v"(kv[u][9]), "+v"(kv[u][10]), "+v"(kv[u][11]), "+v"(kv[u][12]), "+v"(kv[u][13]), "+v"(kv[u][14]), "+v"(kv[u][15])
                   : "i"(u == 0 ? 16 : 0) : "memory");
      char* kl = smem + (c * 2 + u) * 8192 + (lane >> 4) * 256;
#pragma unroll
      for (int i = 0; i < 8; ++i) {
        *(u32x4*)(kl + i * 1024 + (((lane & 15) ^ ((4 * i + (lane >> 4)) & 15)) << 4)) = kv[u][i];
        *(u32x4*)(kl + 65536 + i * 1024 + (((lane & 15) ^ ((lane >> 4) << 2)) << 4)) = kv[u][8 + i];
      }
    }
    asm volatile("s_waitcnt lgkmcnt(0)" ::: "memory");
    __builtin_amdgcn_s_barrier();
    MEMFENCE();
    const u32x4* qs = (const u32x4*)(smem + 131072 + c * 8192) + lane;
#pragma unroll
    for (int i = 0; i < 8; ++i) qf[i] = __builtin_bit_cast(bf16x8, qs[i * 64]);
  }

  const int unit = c * 2 + (w >> 2), b = bg * 8 + unit;
  unsigned pfd;
  {
    const char* pw = (const char*)((bid & 1) ? woB : woA) + (size_t)(bid >> 1) * 32768 + w * 4096;
    asm volatile("global_load_dword %0, %1, %2" : "=v"(pfd) : "v"(lane * 64), "s"(pw) : "memory");
  }
  const char* kl = smem + unit * 8192;
  const char* vl = kl + 65536;
  const int pr = (r & 0x13) | ((r & 4) << 1) | ((r & 8) >> 1);
  f32x16 sacc, sac1;
#pragma unroll
  for (int i = 0; i < 16; ++i) { sacc[i] = 0.f; sac1[i] = 0.f; }
#pragma unroll
  for (int i = 0; i < 8; i += 2) {
    sacc = MFMA32(*(const bf16x8*)(kl + pr * 256 + (((2 * i + hh) ^ (pr & 15)) << 4)), qf[i], sacc);
    sac1 = MFMA32(*(const bf16x8*)(kl + pr * 256 + (((2 * i + 2 + hh) ^ (pr & 15)) << 4)), qf[i + 1], sac1);
  }
  sacc += sac1;
  STAMP(sb, 3);

  const float scale = 0.08838834764831845f;
  float m = -3.0e38f;
#pragma unroll
  for (int i = 0; i < 16; i += 2) {
    unsigned u = cvtpk(sacc[i], sacc[i + 1]);
    float a = __uint_as_float(u << 16) * scale, cc = __uint_as_float(u & 0xffff0000u) * scale;
    unsigned u2 = cvtpk(a, cc);
    sacc[i] = __uint_as_float(u2 << 16);
    sacc[i + 1] = __uint_as_float(u2 & 0xffff0000u);
    m = fmaxf(m, fmaxf(sacc[i], sacc[i + 1]));
  }
  {
    auto rr = __builtin_amdgcn_permlane32_swap(__float_as_uint(m), __float_as_uint(m), false, false);
    m = fmaxf(__uint_as_float(rr[0]), __uint_as_float(rr[1]));
  }
  float sum = 0.f;
#pragma unroll
  for (int i = 0; i < 16; ++i) {
    sacc[i] = __builtin_amdgcn_exp2f((sacc[i] - m) * 1.4426950408889634f);
    sum += sacc[i];
  }
  {
    auto rr = __builtin_amdgcn_permlane32_swap(__float_as_uint(sum), __float_as_uint(sum), false, false);
    sum = __uint_as_float(rr[0]) + __uint_as_float(rr[1]);
  }
  const float inv = 1.0f / sum;
#pragma unroll
  for (int i = 0; i < 16; ++i) sacc[i] *= inv;
  bf16x8 pf0 = pack8(sacc, 0), pf1 = pack8(sacc, 1);

  u32x4* od = (u32x4*)(ows + ((size_t)((X * 32 + b) * 256 + h * 8) * 64 + lane) * 8);
#pragma unroll
  for (int t = 0; t < 4; ++t) {
    f32x16 o;
#pragma unroll
    for (int i = 0; i < 16; ++i) o[i] = 0.f;
    const char* pa = vl + tro[t];
    o = MFMA32(tr_frag(pa, pa + 1024), pf0, o);
    o = MFMA32(tr_frag(pa + 4096, pa + 4096 + 1024), pf1, o);
    od[(2 * t) * 64] = __builtin_bit_cast(u32x4, pack8(o, 0));
    od[(2 * t + 1) * 64] = __builtin_bit_cast(u32x4, pack8(o, 1));
  }
  STAMP(sb, 4);
  asm volatile("s_waitcnt vmcnt(0)" : "+v"(pfd)::"memory");
  STAMP(sb, 5);
}

constexpr int K2_LDS = 131072;

__global__ __launch_bounds__(512, 2) void k2_oproj(const uint16_t* __restrict__ woA, const uint16_t* __restrict__ woB,
                                                   const uint16_t* __restrict__ ows, uint16_t* __restrict__ out) {
  extern __shared__ __attribute__((aligned(16))) char smem[];
  const int tid = threadIdx.x, lane = tid & 63;
  const int w = __builtin_amdgcn_readfirstlane(tid >> 6);
  const int bid = blockIdx.x, xcd = bid & 7, idx = bid >> 3;
  const int X = xcd >> 2, rp = (xcd & 3) * 4 + (idx >> 3), nc = idx & 7;
  const uint16_t* wo = X ? woB : woA;
  unsigned long long* sb = (unsigned long long*)((char*)ows + (65u << 20)); (void)sb;
  STAMP(sb, 0);
  const int r = lane & 31, hh = lane >> 5, blk = (lane >> 4) & 1, q = (lane & 15) >> 2, p = lane & 3;

  const char* wb = (const char*)(wo + (size_t)(w * 512) * 512 + nc * 64);
  const unsigned woff = (lane >> 3) * 1024 + (((lane & 7) ^ (((lane >> 4) & 1) << 2)) << 4);
  const char* lw = smem + w * 16384;
  const unsigned lwa = lds_addr(smem) + w * 16384;
  const char* ob = (const char*)ows + (size_t)((X * 32 + rp * 2) * 256 + w * 32) * 1024;
  const unsigned oo0 = lane * 16, oo1 = lane * 16 + 262144;

  u32x4 of[8][2];
  f32x16 acc[2][2];
#pragma unroll
  for (int a = 0; a < 2; ++a)
#pragma unroll
    for (int c = 0; c < 2; ++c)
#pragma unroll
      for (int i = 0; i < 16; ++i) acc[a][c][i] = 0.f;

#define K2_ROT 0
  const int rot = K2_ROT;
  auto piece = [&](int ks_, int j) {
    const int slot = ks_ & 7, ks = (ks_ + rot) & 31;
    if (j == 0) glds16<NT_WO>(wb + (size_t)ks * 16384, woff, lwa + slot * 2048);
    if (j == 1) glds16<NT_WO>(wb + (size_t)ks * 16384 + 8192, woff, lwa + slot * 2048 + 1024);
    if (j == 2) asm volatile("global_load_dwordx4 %0, %1, %2" : "=v"(of[slot][0]) : "v"(oo0), "s"(ob + ks * 1024) : "memory");
    if (j == 3) asm volatile("global_load_dwordx4 %0, %1, %2" : "=v"(of[slot][1]) : "v"(oo1), "s"(ob + ks * 1024) : "memory");
  };

  const int troff = (8 * hh + q) * 128 + 32 * blk + 16 * (p & 1) + 8 * (p >> 1);
  const int hsw = (q >> 1) & 1;
  const char* pa0 = lw + troff + ((0 ^ hsw) << 6);
  const char* pa1 = lw + troff + ((1 ^ hsw) << 6);
  bf16x8 af[2][2];
  auto ldfrag = [&](int ks) {
    const int o = (ks & 7) * 2048;
    af[ks & 1][0] = tr_frag(pa0 + o, pa0 + o + 512);
    af[ks & 1][1] = tr_frag(pa1 + o, pa1 + o + 512);
  };
#define K2_WAIT(N, ks) asm volatile("s_waitcnt vmcnt(%2)" : "+v"(of[(ks) & 7][0]), "+v"(of[(ks) & 7][1]) : "i"(N) : "memory")

#pragma unroll
  for (int ks = 0; ks < 7; ++ks)
#pragma unroll
    for (int j = 0; j < 4; ++j) piece(ks, j);
  K2_WAIT(24, 0);
  STAMP(sb, 1);
  ldfrag(0);

#pragma unroll
  for (int ks = 0; ks < 32; ++ks) {
    if (ks < 31) {
      constexpr int dummy = 0; (void)dummy;
      const int last = (ks + 6 < 31) ? ks + 6 : 31;
      switch (last - (ks + 1)) {
        case 5: K2_WAIT(20, ks + 1); break;
        case 4: K2_WAIT(16, ks + 1); break;
        case 3: K2_WAIT(12, ks + 1); break;
        case 2: K2_WAIT(8, ks + 1); break;
        case 1: K2_WAIT(4, ks + 1); break;
        default: K2_WAIT(0, ks + 1); break;
      }
    }
    const bf16x8 b0 = __builtin_bit_cast(bf16x8, of[ks & 7][0]), b1 = __builtin_bit_cast(bf16x8, of[ks & 7][1]);
    const bool pre = ks + 7 < 32;
    acc[0][0] = MFMA32(af[ks & 1][0], b0, acc[0][0]);
    SCHED0();
    if (ks < 31) ldfrag(ks + 1);
    if (pre) piece(ks + 7, 0);
    SCHED0();
    acc[0][1] = MFMA32(af[ks & 1][0], b1, acc[0][1]);
    SCHED0();
    if (pre) piece(ks + 7, 1);
    SCHED0();
    acc[1][0] = MFMA32(af[ks & 1][1], b0, acc[1][0]);
    SCHED0();
    if (pre) piece(ks + 7, 2);
    SCHED0();
    acc[1][1] = MFMA32(af[ks & 1][1], b1, acc[1][1]);
    SCHED0();
    if (pre) piece(ks + 7, 3);
    SCHED0();
  }
  STAMP(sb, 2);

  {
    f32x4* rd = (f32x4*)(smem + w * 16384) + lane;
#pragma unroll
    for (int nt = 0; nt < 2; ++nt)
#pragma unroll
      for (int bt = 0; bt < 2; ++bt)
#pragma unroll
        for (int g = 0; g < 4; ++g) {
          f32x4 v = {acc[nt][bt][4 * g], acc[nt][bt][4 * g + 1], acc[nt][bt][4 * g + 2], acc[nt][bt][4 * g + 3]};
          rd[((nt * 2 + bt) * 4 + g) * 64] = v;
        }
  }
  __syncthreads();
  {
    const f32x4* rs = (const f32x4*)smem + (w * 2) * 64 + lane;
    f32x4 s0 = rs[0], s1 = rs[64];
#pragma unroll
    for (int j = 1; j < 8; ++j) {
      s0 += rs[j * 1024];
      s1 += rs[j * 1024 + 64];
    }
    const int tile = w >> 1, nt = tile >> 1, bt = tile & 1, s = w & 1;
    u32x4 o = {cvtpk(s0[0], s0[1]), cvtpk(s0[2], s0[3]), cvtpk(s1[0], s1[1]), cvtpk(s1[2], s1[3])};
    uint16_t* dst = out + (size_t)(rp * 64 + bt * 32 + r) * 1024 + X * 512 + nc * 64 + nt * 32 + s * 16 + hh * 8;
    *(u32x4*)dst = o;
  }
  STAMP(sb, 4);
  STAMP(sb, 5);
}

extern "C" void kernel_launch(void* const* d_in, const int* in_sizes, int n_in, void* d_out, int out_size,
                              void* d_ws, size_t ws_size, hipStream_t stream) {
  (void)in_sizes; (void)n_in; (void)out_size; (void)ws_size;
  (void)hipFuncSetAttribute((const void*)k1_qproj_attn, hipFuncAttributeMaxDynamicSharedMemorySize, K1_LDS);
  (void)hipFuncSetAttribute((const void*)k2_oproj, hipFuncAttributeMaxDynamicSharedMemorySize, K2_LDS);
  const uint16_t* xA = (const uint16_t*)d_in[0];
  const uint16_t* kA = (const uint16_t*)d_in[1];
  const uint16_t* vA = (const uint16_t*)d_in[2];
  const uint16_t* wqA = (const uint16_t*)d_in[3];
  const uint16_t* woA = (const uint16_t*)d_in[4];
  const uint16_t* xB = (const uint16_t*)d_in[5];
  const uint16_t* kB = (const uint16_t*)d_in[6];
  const uint16_t* vB = (const uint16_t*)d_in[7];
  const uint16_t* wqB = (const uint16_t*)d_in[8];
  const uint16_t* woB = (const uint16_t*)d_in[9];
  uint16_t* ows = (uint16_t*)d_ws;
  k1_qproj_attn<<<256, 512, K1_LDS, stream>>>(xA, kA, vA, wqA, xB, kB, vB, wqB, ows, woA, woB);
  k2_oproj<<<256, 512, K2_LDS, stream>>>(woA, woB, ows, (uint16_t*)d_out);
}
```

```cpp
#include <hip/hip_runtime.h>
#include <hip/hip_bf16.h>
#include <stdint.h>

typedef __attribute__((ext_vector_type(8))) __bf16 bf16x8;
typedef __attribute__((ext_vector_type(2))) __bf16 bf16x2;
typedef __attribute__((ext_vector_type(4))) short s16x4;
typedef __attribute__((ext_vector_type(2))) float f32x2;
typedef __attribute__((ext_vector_type(4))) float f32x4;
typedef __attribute__((ext_vector_type(16))) float f32x16;
typedef __attribute__((ext_vector_type(4))) unsigned u32x4;
typedef __attribute__((address_space(3))) s16x4* lds_s16x4_ptr;

#define DI __device__ __forceinline__
#define MFMA32(a, b, c) __builtin_amdgcn_mfma_f32_32x32x16_bf16((a), (b), (c), 0, 0, 0)
#define MEMFENCE() asm volatile("" ::: "memory")
#define SCHED0() __builtin_amdgcn_sched_barrier(0)
#define USE_PRIO 1
#define PRIO(n) __builtin_amdgcn_s_setprio(n)

#define NT_KV 1
#define RDFIRST 1
#define OST 2
#define PF_XCD 1
#define OSTS "sc1"
#define KVFRONT 0
#define PF_WO 1
#define EARLY_RD true
#define K1_ILV 1
#define PF_WO_ST 7
#define USE_PF 0
#define USE_ROT 0
#define NT_WQ 1
#define NT_X 0
#define NT_WO 0

DI unsigned cvtpk(float lo, float hi) {
  f32x2 v = {lo, hi};
  bf16x2 r = __builtin_convertvector(v, bf16x2);
  return __builtin_bit_cast(unsigned, r);
}
DI bf16x8 pack8(const f32x16& x, int s) {
  u32x4 p = {cvtpk(x[8 * s + 0], x[8 * s + 1]), cvtpk(x[8 * s + 2], x[8 * s + 3]),
             cvtpk(x[8 * s + 4], x[8 * s + 5]), cvtpk(x[8 * s + 6], x[8 * s + 7])};
  return __builtin_bit_cast(bf16x8, p);
}
DI s16x4 tr_read(const char* p) { return __builtin_amdgcn_ds_read_tr16_b64_v4i16((lds_s16x4_ptr)(p)); }
DI bf16x8 tr_frag(const char* p0, const char* p1) {
  s16x4 lo = tr_read(p0), hi = tr_read(p1);
  return __builtin_bit_cast(bf16x8, __builtin_shufflevector(lo, hi, 0, 1, 2, 3, 4, 5, 6, 7));
}
template <int NT>
DI void glds16(const void* gbase, unsigned voff, unsigned ldst) {
  unsigned keep;
  if constexpr (NT)
    asm volatile("s_mov_b32 %0, m0\n\ts_mov_b32 m0, %2\n\ts_nop 0\n\tglobal_load_lds_dwordx4 %1, %3 nt\n\ts_mov_b32 m0, %0"
                 : "=&s"(keep) : "v"(voff), "s"(ldst), "s"(gbase) : "memory");
  else
    asm volatile("s_mov_b32 %0, m0\n\ts_mov_b32 m0, %2\n\ts_nop 0\n\tglobal_load_lds_dwordx4 %1, %3\n\ts_mov_b32 m0, %0"
                 : "=&s"(keep) : "v"(voff), "s"(ldst), "s"(gbase) : "memory");
}
DI void pf4(const void* gbase, unsigned voff, unsigned ldst) {
  unsigned keep;
  asm volatile("s_mov_b32 %0, m0\n\ts_mov_b32 m0, %2\n\ts_nop 0\n\tglobal_load_lds_dword %1, %3\n\ts_mov_b32 m0, %0"
               : "=&s"(keep) : "v"(voff), "s"(ldst), "s"(gbase) : "memory");
}
#define PF_STRIDE 128
DI unsigned lds_addr(const void* p) { return (unsigned)(size_t)(__attribute__((address_space(3))) const char*)p; }
template <int A, int B> DI void wait_vm_sel(int cond) {
  asm volatile("s_waitcnt vmcnt(%2) lgkmcnt(0)\n\ts_cmp_eq_u32 %0, 0\n\ts_cbranch_scc1 .Lw%=\n\ts_waitcnt vmcnt(%1)\n\t.Lw%=:"
               ::"s"(cond), "i"(A), "i"(B) : "memory", "scc");
}
#define STAMP(buf, i)
template <int N> DI void wait_vm() { asm volatile("s_waitcnt vmcnt(%0) lgkmcnt(0)" ::"i"(N) : "memory"); }

constexpr int K1_STAGE = 49152;
constexpr int K1_LDS = 163840;

__global__ __launch_bounds__(512, 2) void k1_qproj_attn(
    const uint16_t* __restrict__ xA, const uint16_t* __restrict__ kA, const uint16_t* __restrict__ vA,
    const uint16_t* __restrict__ wqA, const uint16_t* __restrict__ xB, const uint16_t* __restrict__ kB,
    const uint16_t* __restrict__ vB, const uint16_t* __restrict__ wqB, uint16_t* __restrict__ ows,
    const uint16_t* __restrict__ woA, const uint16_t* __restrict__ woB, unsigned* __restrict__ cnt) {
  extern __shared__ __attribute__((aligned(16))) char smem[];
  const int tid = threadIdx.x, lane = tid & 63;
  const int w = __builtin_amdgcn_readfirstlane(tid >> 6);
  const int bid = blockIdx.x, xcd = bid & 7, idx = bid >> 3;
  const int X = xcd >> 2, h = (xcd & 3) * 8 + (idx & 7), bg = idx >> 3;
  const int c = w & 3;
  if (tid == 0) cnt[bid * 16] = 0u;
  unsigned long long* sb = (unsigned long long*)((char*)ows + (64u << 20)); (void)sb;
  STAMP(sb, 0);
  const int r = lane & 31, hh = lane >> 5, blk = (lane >> 4) & 1, q = (lane & 15) >> 2, p = lane & 3;
  const int troff = (8 * hh + q) * 256 + 32 * blk + 16 * (p & 1) + 8 * (p >> 1);
  int tro[4];
#pragma unroll
  for (int t = 0; t < 4; ++t) tro[t] = troff + ((t ^ q) << 6);
  bf16x8 qf[8];
  unsigned pfk0 = 0, pfk1 = 0;

  if (w < 4) {
    f32x16 acc[4][2];
#pragma unroll
    for (int t = 0; t < 4; ++t)
#pragma unroll
      for (int u = 0; u < 2; ++u)
#pragma unroll
        for (int i = 0; i < 16; ++i) acc[t][u][i] = 0.f;
    const int f = (r >> 1) & 7;
    int xo[4];
#pragma unroll
    for (int s = 0; s < 4; ++s) xo[s] = 16384 + c * 8192 + r * 128 + (((2 * s + hh) ^ f) << 4);
    bf16x8 af[2][4], bf[2][2];
    auto rdA = [&](const char* lb, int s, int slot, int t) {
      af[slot][t] = tr_frag(lb + tro[t] + s * 4096, lb + tro[t] + s * 4096 + 1024);
    };
    auto rdB = [&](const char* lb, int s, int slot) {
      bf[slot][0] = *(const bf16x8*)(lb + xo[s]);
      bf[slot][1] = *(const bf16x8*)(lb + xo[s] + 4096);
    };
    auto kstep = [&](int slot, const char* lb, int s) {
      const int ns = slot ^ 1;
      acc[0][0] = MFMA32(af[slot][0], bf[slot][0], acc[0][0]); SCHED0();
      rdB(lb, s, ns); SCHED0();
      acc[0][1] = MFMA32(af[slot][0], bf[slot][1], acc[0][1]); SCHED0();
      rdA(lb, s, ns, 0); SCHED0();
      acc[1][0] = MFMA32(af[slot][1], bf[slot][0], acc[1][0]); SCHED0();
      rdA(lb, s, ns, 1); SCHED0();
      acc[1][1] = MFMA32(af[slot][1], bf[slot][1], acc[1][1]); SCHED0();
      rdA(lb, s, ns, 2); SCHED0();
      acc[2][0] = MFMA32(af[slot][2], bf[slot][0], acc[2][0]); SCHED0();
      rdA(lb, s, ns, 3); SCHED0();
      acc[2][1] = MFMA32(af[slot][2], bf[slot][1], acc[2][1]);
      acc[3][0] = MFMA32(af[slot][3], bf[slot][0], acc[3][0]);
      acc[3][1] = MFMA32(af[slot][3], bf[slot][1], acc[3][1]); SCHED0();
    };
    __builtin_amdgcn_s_barrier();
    MEMFENCE();
    STAMP(sb, 1);
    rdB(smem, 0, 0);
#pragma unroll
    for (int t = 0; t < 4; ++t) rdA(smem, 0, 0, t);
    SCHED0();
    unsigned cur = 0;
#pragma unroll 1
    for (int st = 0; st < 8; ++st) {
      const unsigned nxt = (cur == 2 * K1_STAGE) ? 0u : cur + K1_STAGE;
      const char* lb = smem + cur;
      kstep(0, lb, 1);
      kstep(1, lb, 2);
      kstep(0, lb, 3);
      asm volatile("s_waitcnt lgkmcnt(0)" ::: "memory");
      __builtin_amdgcn_s_barrier();
      MEMFENCE();
      kstep(1, smem + nxt, 0);
      cur = nxt;
    }
    STAMP(sb, 2);
    u32x4* qd = (u32x4*)(smem + 65536 + c * 8192) + lane;
#pragma unroll
    for (int t = 0; t < 4; ++t)
#pragma unroll
      for (int s = 0; s < 2; ++s) {
        qd[(2 * t + s) * 64] = __builtin_bit_cast(u32x4, pack8(acc[t][1], s));
        qf[2 * t + s] = pack8(acc[t][0], s);
      }
    asm volatile("s_waitcnt lgkmcnt(0)" ::: "memory");
    __builtin_amdgcn_s_barrier();
    MEMFENCE();
  } else {
    const uint16_t* x = X ? xB : xA;
    const uint16_t* kc = X ? kB : kA;
    const uint16_t* vc = X ? vB : vA;
    const uint16_t* wq = X ? wqB : wqA;
    const char* xb = (const char*)(x + (size_t)((bg * 8 + c * 2) * 32) * 512);
    const char* wb = (const char*)(wq + (size_t)(c * 16) * 4096 + h * 128);
    const unsigned xoff0 = (lane >> 3) * 1024 + (((lane & 7) ^ (lane >> 4)) << 4);
    const unsigned xoff1 = (8 + (lane >> 3)) * 1024 + (((lane & 7) ^ ((lane >> 4) | 4)) << 4);
    const unsigned woff = (lane >> 4) * 8192 + (((lane & 15) ^ ((lane >> 4) << 2)) << 4);
    const unsigned sbase = lds_addr(smem);
    auto stage = [&](int st, unsigned bufoff) {
      const unsigned lb = sbase + bufoff;
      const char* wg = wb + (size_t)st * (64 * 8192);
#pragma unroll
      for (int j = 0; j < 4; ++j) glds16<NT_WQ>(wg + j * (4 * 8192), woff, lb + c * 4096 + j * 1024);
      const unsigned lx = lb + 16384 + c * 8192;
      const char* xg = xb + st * 128;
#pragma unroll
      for (int j = 0; j < 4; ++j) {
        glds16<NT_X>(xg + j * 16384, xoff0, lx + j * 2048);
        glds16<NT_X>(xg + j * 16384, xoff1, lx + j * 2048 + 1024);
      }
    };
    const unsigned kvo = lane * 16;
    u32x4 kv[2][16];
#define KVNT " nt"
    auto kvchunk = [&](int ch) {
      const int isv = ch >> 2, u = (ch >> 1) & 1, o = isv * 8 + (ch & 1) * 4;
      const uint16_t* g = (isv ? vc : kc) + (size_t)((bg * 8 + c * 2 + u) * 32 + h) * 4096 + (ch & 1) * 2048;
      asm volatile(
          "global_load_dwordx4 %0, %4, %5" KVNT "\n\t"
          "global_load_dwordx4 %1, %4, %5 offset:1024" KVNT "\n\t"
          "global_load_dwordx4 %2, %4, %5 offset:2048" KVNT "\n\t"
          "global_load_dwordx4 %3, %4, %5 offset:3072" KVNT
          : "=v"(kv[u][o]), "=v"(kv[u][o + 1]), "=v"(kv[u][o + 2]), "=v"(kv[u][o + 3])
          : "v"(kvo), "s"(g) : "memory");
    };
#define KV_PIN(u, o, N) asm volatile("s_waitcnt vmcnt(%8)" : "+v"(kv[u][o]), "+v"(kv[u][o + 1]), "+v"(kv[u][o + 2]), "+v"(kv[u][o + 3]), \
                                     "+v"(kv[u][o + 4]), "+v"(kv[u][o + 5]), "+v"(kv[u][o + 6]), "+v"(kv[u][o + 7]) : "i"(N) : "memory")
    auto putK = [&](int u) {
      char* kl = smem + 98304 + (c * 2 + u) * 8192 + (lane >> 4) * 256;
#pragma unroll
      for (int i = 0; i < 8; ++i) *(u32x4*)(kl + i * 1024 + (((lane & 15) ^ ((4 * i + (lane >> 4)) & 15)) << 4)) = kv[u][i];
    };
    auto putV = [&](int u) {
      char* vl = smem + u * 32768 + c * 8192 + (lane >> 4) * 256 + (((lane & 15) ^ ((lane >> 4) << 2)) << 4);
#pragma unroll
      for (int i = 0; i < 8; ++i) *(u32x4*)(vl + i * 1024) = kv[u][8 + i];
    };
    stage(0, 0);
    stage(1, K1_STAGE);
    stage(2, 2 * K1_STAGE);
    kvchunk(0);
    kvchunk(1);
    asm volatile("s_waitcnt vmcnt(32)" ::: "memory");
    __builtin_amdgcn_s_barrier();
    MEMFENCE();
    unsigned pf0 = 0, pf1 = 0;
#pragma unroll
    for (int st = 0; st < 8; ++st) {
      if (st == 1) asm volatile("s_waitcnt vmcnt(24)" ::: "memory");
      else if (st < 6) asm volatile("s_waitcnt vmcnt(20)" ::: "memory");
      else if (st == 6) asm volatile("s_waitcnt vmcnt(8)" ::: "memory");
      __builtin_amdgcn_s_barrier();
      MEMFENCE();
      if (st < 5) stage(st + 3, (st % 3) * K1_STAGE);
      if (st < 6) kvchunk(st + 2);
      if (st == 5) {
        KV_PIN(0, 0, 24); KV_PIN(1, 0, 24);
        putK(0); putK(1);
      }
      if (st == 6) {
        if (PF_WO) {
          const char* pw = (const char*)(X ? woB : woA) + (size_t)((xcd & 3) * 32 + idx) * 32768 + c * 8192;
          asm volatile("global_load_dword %0, %2, %3\n\tglobal_load_dword %1, %2, %3 offset:64"
                       : "=v"(pf0), "=v"(pf1) : "v"(lane * 128), "s"(pw) : "memory");
          KV_PIN(0, 8, 10);
        } else {
          KV_PIN(0, 8, 8);
        }
        putV(0);
      }
    }
    if (PF_WO) KV_PIN(1, 8, 2); else KV_PIN(1, 8, 0);
    putV(1);
    pfk0 = pf0; pfk1 = pf1;
    asm volatile("s_waitcnt lgkmcnt(0)" ::: "memory");
    __builtin_amdgcn_s_barrier();
    MEMFENCE();
    const u32x4* qs = (const u32x4*)(smem + 65536 + c * 8192) + lane;
#pragma unroll
    for (int i = 0; i < 8; ++i) qf[i] = __builtin_bit_cast(bf16x8, qs[i * 64]);
  }

  const int unit = c * 2 + (w >> 2), b = bg * 8 + unit;
  const char* kl = smem + 98304 + unit * 8192;
  const char* vl = smem + (w >> 2) * 32768 + c * 8192;
  const int pr = (r & 0x13) | ((r & 4) << 1) | ((r & 8) >> 1);
  f32x16 sacc, sac1;
#pragma unroll
  for (int i = 0; i < 16; ++i) { sacc[i] = 0.f; sac1[i] = 0.f; }
#pragma unroll
  for (int i = 0; i < 8; i += 2) {
    sacc = MFMA32(*(const bf16x8*)(kl + pr * 256 + (((2 * i + hh) ^ (pr & 15)) << 4)), qf[i], sacc);
    sac1 = MFMA32(*(const bf16x8*)(kl + pr * 256 + (((2 * i + 2 + hh) ^ (pr & 15)) << 4)), qf[i + 1], sac1);
  }
  sacc += sac1;
  STAMP(sb, 3);

  const float scale = 0.08838834764831845f;
  float m = -3.0e38f;
#pragma unroll
  for (int i = 0; i < 16; i += 2) {
    unsigned u = cvtpk(sacc[i], sacc[i + 1]);
    float a = __uint_as_float(u << 16) * scale, cc = __uint_as_float(u & 0xffff0000u) * scale;
    unsigned u2 = cvtpk(a, cc);
    sacc[i] = __uint_as_float(u2 << 16);
    sacc[i + 1] = __uint_as_float(u2 & 0xffff0000u);
    m = fmaxf(m, fmaxf(sacc[i], sacc[i + 1]));
  }
  {
    auto rr = __builtin_amdgcn_permlane32_swap(__float_as_uint(m), __float_as_uint(m), false, false);
    m = fmaxf(__uint_as_float(rr[0]), __uint_as_float(rr[1]));
  }
  float sum = 0.f;
#pragma unroll
  for (int i = 0; i < 16; ++i) {
    sacc[i] = __builtin_amdgcn_exp2f((sacc[i] - m) * 1.4426950408889634f);
    sum += sacc[i];
  }
  {
    auto rr = __builtin_amdgcn_permlane32_swap(__float_as_uint(sum), __float_as_uint(sum), false, false);
    sum = __uint_as_float(rr[0]) + __uint_as_float(rr[1]);
  }
  const float inv = 1.0f / sum;
#pragma unroll
  for (int i = 0; i < 16; ++i) sacc[i] *= inv;
  bf16x8 pf0 = pack8(sacc, 0), pf1 = pack8(sacc, 1);

  u32x4* od = (u32x4*)(ows + ((size_t)((X * 32 + b) * 256 + h * 8) * 64 + lane) * 8);
#pragma unroll
  for (int t = 0; t < 4; ++t) {
    f32x16 o;
#pragma unroll
    for (int i = 0; i < 16; ++i) o[i] = 0.f;
    const char* pa = vl + tro[t];
    o = MFMA32(tr_frag(pa, pa + 1024), pf0, o);
    o = MFMA32(tr_frag(pa + 4096, pa + 4096 + 1024), pf1, o);
    {
      u32x4 v0 = __builtin_bit_cast(u32x4, pack8(o, 0)), v1 = __builtin_bit_cast(u32x4, pack8(o, 1));
      asm volatile("global_store_dwordx4 %0, %1, off " OSTS "\n\tglobal_store_dwordx4 %0, %2, off offset:1024 " OSTS "\n\ts_nop 1"
                   ::"v"(od + (2 * t) * 64), "v"(v0), "v"(v1) : "memory");
    }
  }
  STAMP(sb, 4);
  asm volatile("s_waitcnt vmcnt(0)" : "+v"(pfk0), "+v"(pfk1)::"memory");
  STAMP(sb, 5);
}

constexpr int K2_NB = 8;
constexpr int K2_LDS = K2_NB * 16384;
#define K2_OD 16

__global__ __launch_bounds__(512, 2) void k2_oproj(const uint16_t* __restrict__ woA, const uint16_t* __restrict__ woB,
                                                   const uint16_t* __restrict__ ows, float* __restrict__ slab,
                                                   unsigned* __restrict__ cnt, uint16_t* __restrict__ out) {
  extern __shared__ __attribute__((aligned(16))) char smem[];
  const int tid = threadIdx.x, lane = tid & 63;
  const int w = __builtin_amdgcn_readfirstlane(tid >> 6);
  const int bid = blockIdx.x, xcd = bid & 7, idx = bid >> 3;
  const int X = xcd >> 2, hg = xcd & 3, rt = idx >> 2, ct = idx & 3;
  const int c = w & 3;
  const int tile = (X * 8 + rt) * 4 + ct;
  unsigned long long* sb = (unsigned long long*)((char*)ows + (65u << 20)); (void)sb;
  STAMP(sb, 0);

  if (w < 4) {
    const int hh = lane >> 5, blk = (lane >> 4) & 1, q = (lane & 15) >> 2, p = lane & 3;
    const int troff = (8 * hh + q) * 256 + 32 * blk + 16 * (p & 1) + 8 * (p >> 1);
    unsigned pa[2][4];
#pragma unroll
    for (int t = 0; t < 4; ++t) {
      pa[0][t] = lds_addr(smem) + troff + ((t ^ q) << 6);
      pa[1][t] = pa[0][t] + 65536;
      asm volatile("" : "+v"(pa[0][t]), "+v"(pa[1][t]));
    }
    const char* ob = (const char*)ows + (size_t)((X * 32 + rt * 4 + c) * 256 + hg * 64) * 1024;
    const unsigned oo = lane * 16;
    u32x4 of[K2_OD];
    f32x16 acc[4];
#pragma unroll
    for (int t = 0; t < 4; ++t)
#pragma unroll
      for (int i = 0; i < 16; ++i) acc[t][i] = 0.f;
    bf16x8 af[2][4];
    auto oload = [&](int ks) {
      asm volatile("global_load_dwordx4 %0, %1, %2" : "=v"(of[ks % K2_OD]) : "v"(oo), "s"(ob + ks * 1024) : "memory");
    };
    auto rdA = [&](int ks, int t) {
      const int o = ((ks >> 2) % K2_NB) * 16384 + (ks & 3) * 4096;
      const unsigned a = pa[o >> 16][t] + (o & 65535);
      s16x4 lo = __builtin_amdgcn_ds_read_tr16_b64_v4i16((lds_s16x4_ptr)(size_t)a);
      s16x4 hi = __builtin_amdgcn_ds_read_tr16_b64_v4i16((lds_s16x4_ptr)(size_t)(a + 1024));
      af[ks & 1][t] = __builtin_bit_cast(bf16x8, __builtin_shufflevector(lo, hi, 0, 1, 2, 3, 4, 5, 6, 7));
    };
#pragma unroll
    for (int ks = 0; ks < K2_OD; ++ks) oload(ks);
    __builtin_amdgcn_s_barrier();
    MEMFENCE();
    STAMP(sb, 1);
#pragma unroll
    for (int t = 0; t < 4; ++t) rdA(0, t);
    SCHED0();
#pragma unroll
    for (int ks = 0; ks < 64; ++ks) {
      if ((ks & 3) == 3 && ks < 63) {
        asm volatile("s_waitcnt lgkmcnt(0)" ::: "memory");
        __builtin_amdgcn_s_barrier();
        MEMFENCE();
      }
      {
        constexpr int dummy = 0; (void)dummy;
        const int n = (63 - ks < K2_OD - 1) ? 63 - ks : K2_OD - 1;
        asm volatile("s_waitcnt vmcnt(%1)" : "+v"(of[ks % K2_OD]) : "i"(n) : "memory");
      }
      const bf16x8 bfr = __builtin_bit_cast(bf16x8, of[ks % K2_OD]);
      acc[0] = MFMA32(af[ks & 1][0], bfr, acc[0]); SCHED0();
      if (ks < 63) { rdA(ks + 1, 0); rdA(ks + 1, 1); } SCHED0();
      acc[1] = MFMA32(af[ks & 1][1], bfr, acc[1]); SCHED0();
      if (ks < 63) { rdA(ks + 1, 2); rdA(ks + 1, 3); } SCHED0();
      acc[2] = MFMA32(af[ks & 1][2], bfr, acc[2]);
      acc[3] = MFMA32(af[ks & 1][3], bfr, acc[3]); SCHED0();
      if (ks + K2_OD < 64) oload(ks + K2_OD);
      SCHED0();
    }
    STAMP(sb, 2);
    if (c == hg) {
      f32x4* sd = (f32x4*)smem + lane;
#pragma unroll
      for (int t = 0; t < 4; ++t)
#pragma unroll
        for (int g = 0; g < 4; ++g) sd[(t * 4 + g) * 64] = f32x4{acc[t][4 * g], acc[t][4 * g + 1], acc[t][4 * g + 2], acc[t][4 * g + 3]};
      asm volatile("s_waitcnt lgkmcnt(0)" ::: "memory");
    } else {
      const float* dst = slab + ((size_t)(tile * 4 + c) * 3 + (hg < c ? hg : hg - 1)) * 4096;
      const unsigned so = lane * 16;
#pragma unroll
      for (int t = 0; t < 4; ++t) {
        f32x4 v0 = {acc[t][0], acc[t][1], acc[t][2], acc[t][3]}, v1 = {acc[t][4], acc[t][5], acc[t][6], acc[t][7]};
        f32x4 v2 = {acc[t][8], acc[t][9], acc[t][10], acc[t][11]}, v3 = {acc[t][12], acc[t][13], acc[t][14], acc[t][15]};
        asm volatile("global_store_dwordx4 %0, %2, %1 sc1\n\t"
                     "global_store_dwordx4 %0, %3, %1 offset:1024 sc1\n\t"
                     "global_store_dwordx4 %0, %4, %1 offset:2048 sc1\n\t"
                     "global_store_dwordx4 %0, %5, %1 offset:3072 sc1\n\ts_nop 1"
                     ::"v"(so), "s"(dst + t * 1024), "v"(v0), "v"(v1), "v"(v2), "v"(v3) : "memory");
      }
      asm volatile("s_waitcnt vmcnt(0)" ::: "memory");
      if (lane == 0) __hip_atomic_fetch_add(cnt + (tile * 4 + c) * 16, 1u, __ATOMIC_RELAXED, __HIP_MEMORY_SCOPE_AGENT);
    }
  } else {
    const uint16_t* wo = X ? woB : woA;
    const char* wb = (const char*)(wo + (size_t)(hg * 1024 + c * 16) * 512 + ct * 128);
    const unsigned woff = (lane >> 4) * 1024 + (((lane & 15) ^ ((lane >> 4) << 2)) << 4);
    const unsigned sbase = lds_addr(smem) + c * 4096;
    auto stage = [&](int st) {
#pragma unroll
      for (int j = 0; j < 4; ++j) glds16<NT_WO>(wb + (size_t)st * 65536 + j * 4096, woff, sbase + (st % K2_NB) * 16384 + j * 1024);
    };
#pragma unroll
    for (int st = 0; st < K2_NB; ++st) stage(st);
    asm volatile("s_waitcnt vmcnt(%0)" ::"i"(4 * (K2_NB - 1)) : "memory");
    __builtin_amdgcn_s_barrier();
    MEMFENCE();
#pragma unroll
    for (int st = 0; st < 15; ++st) {
      constexpr int dummy = 0; (void)dummy;
      const int last = (st + K2_NB - 1 < 15) ? st + K2_NB - 1 : 15;
      asm volatile("s_waitcnt vmcnt(%0)" ::"i"(4 * (last - (st + 1))) : "memory");
      __builtin_amdgcn_s_barrier();
      MEMFENCE();
      if (st + K2_NB < 16) stage(st + K2_NB);
    }
    if (w == 4) {
      const unsigned* cp = cnt + (tile * 4 + hg) * 16;
      for (int spin = 0; spin < (1 << 22); ++spin) {
        if (__hip_atomic_load(cp, __ATOMIC_RELAXED, __HIP_MEMORY_SCOPE_AGENT) >= 3u) break;
        __builtin_amdgcn_s_sleep(2);
      }
    }
  }
  MEMFENCE();
  __builtin_amdgcn_s_barrier();
  MEMFENCE();
  STAMP(sb, 3);
  {
    const float* src = slab + (size_t)(tile * 4 + hg) * 3 * 4096 + w * 512;
    const unsigned so = lane * 16;
    f32x4 a0, a1, b0, b1, c0, c1;
    asm volatile("global_load_dwordx4 %0, %6, %7 sc1\n\t"
                 "global_load_dwordx4 %1, %6, %7 offset:1024 sc1\n\t"
                 "global_load_dwordx4 %2, %6, %8 sc1\n\t"
                 "global_load_dwordx4 %3, %6, %8 offset:1024 sc1\n\t"
                 "global_load_dwordx4 %4, %6, %9 sc1\n\t"
                 "global_load_dwordx4 %5, %6, %9 offset:1024 sc1\n\t"
                 "s_waitcnt vmcnt(0)"
                 : "=&v"(a0), "=&v"(a1), "=&v"(b0), "=&v"(b1), "=&v"(c0), "=&v"(c1)
                 : "v"(so), "s"(src), "s"(src + 4096), "s"(src + 8192) : "memory");
    const f32x4* own = (const f32x4*)smem + (w * 2) * 64 + lane;
    const f32x4 o0 = own[0], o1 = own[64];
    f32x4 s0, s1;
    if (hg == 0) { s0 = ((o0 + a0) + b0) + c0; s1 = ((o1 + a1) + b1) + c1; }
    else if (hg == 1) { s0 = ((a0 + o0) + b0) + c0; s1 = ((a1 + o1) + b1) + c1; }
    else if (hg == 2) { s0 = ((a0 + b0) + o0) + c0; s1 = ((a1 + b1) + o1) + c1; }
    else { s0 = ((a0 + b0) + c0) + o0; s1 = ((a1 + b1) + c1) + o1; }
    u32x4 o = {cvtpk(s0[0], s0[1]), cvtpk(s0[2], s0[3]), cvtpk(s1[0], s1[1]), cvtpk(s1[2], s1[3])};
    uint16_t* dst = out + (size_t)((rt * 4 + hg) * 32 + (lane & 31)) * 1024 + X * 512 + ct * 128 + (w >> 1) * 32 + (w & 1) * 16 + (lane >> 5) * 8;
    *(u32x4*)dst = o;
  }
  STAMP(sb, 4);
  STAMP(sb, 5);
}

extern "C" void kernel_launch(void* const* d_in, const int* in_sizes, int n_in, void* d_out, int out_size,
                              void* d_ws, size_t ws_size, hipStream_t stream) {
  (void)in_sizes; (void)n_in; (void)out_size; (void)ws_size;
  (void)hipFuncSetAttribute((const void*)k1_qproj_attn, hipFuncAttributeMaxDynamicSharedMemorySize, K1_LDS);
  (void)hipFuncSetAttribute((const void*)k2_oproj, hipFuncAttributeMaxDynamicSharedMemorySize, K2_LDS);
  const uint16_t* xA = (const uint16_t*)d_in[0];
  const uint16_t* kA = (const uint16_t*)d_in[1];
  const uint16_t* vA = (const uint16_t*)d_in[2];
  const uint16_t* wqA = (const uint16_t*)d_in[3];
  const uint16_t* woA = (const uint16_t*)d_in[4];
  const uint16_t* xB = (const uint16_t*)d_in[5];
  const uint16_t* kB = (const uint16_t*)d_in[6];
  const uint16_t* vB = (const uint16_t*)d_in[7];
  const uint16_t* wqB = (const uint16_t*)d_in[8];
  const uint16_t* woB = (const uint16_t*)d_in[9];
  uint16_t* ows = (uint16_t*)d_ws;
  float* slab = (float*)((char*)d_ws + (16u << 20));
  unsigned* cnt = (unsigned*)((char*)d_ws + (32u << 20));
  k1_qproj_attn<<<256, 512, K1_LDS, stream>>>(xA, kA, vA, wqA, xB, kB, vB, wqB, ows, woA, woB, cnt);
  k2_oproj<<<256, 512, K2_LDS, stream>>>(woA, woB, ows, slab, cnt, (uint16_t*)d_out);
}
```

```cpp
#include <hip/hip_runtime.h>
#include <hip/hip_bf16.h>
#include <stdint.h>

typedef __attribute__((ext_vector_type(8))) __bf16 bf16x8;
typedef __attribute__((ext_vector_type(2))) __bf16 bf16x2;
typedef __attribute__((ext_vector_type(4))) short s16x4;
typedef __attribute__((ext_vector_type(2))) float f32x2;
typedef __attribute__((ext_vector_type(4))) float f32x4;
typedef __attribute__((ext_vector_type(16))) float f32x16;
typedef __attribute__((ext_vector_type(4))) unsigned u32x4;
typedef __attribute__((address_space(3))) s16x4* lds_s16x4_ptr;

#define DI __device__ __forceinline__
#define MFMA32(a, b, c) __builtin_amdgcn_mfma_f32_32x32x16_bf16((a), (b), (c), 0, 0, 0)
#define MEMFENCE() asm volatile("" ::: "memory")
#define SCHED0() __builtin_amdgcn_sched_barrier(0)

DI unsigned cvtpk(float lo, float hi) {
  f32x2 v = {lo, hi};
  bf16x2 r = __builtin_convertvector(v, bf16x2);
  return __builtin_bit_cast(unsigned, r);
}
DI bf16x8 pack8(const f32x16& x, int s) {
  u32x4 p = {cvtpk(x[8 * s + 0], x[8 * s + 1]), cvtpk(x[8 * s + 2], x[8 * s + 3]),
             cvtpk(x[8 * s + 4], x[8 * s + 5]), cvtpk(x[8 * s + 6], x[8 * s + 7])};
  return __builtin_bit_cast(bf16x8, p);
}
DI s16x4 tr_read(const char* p) { return __builtin_amdgcn_ds_read_tr16_b64_v4i16((lds_s16x4_ptr)(p)); }
DI bf16x8 tr_frag(const char* p0, const char* p1) {
  s16x4 lo = tr_read(p0), hi = tr_read(p1);
  return __builtin_bit_cast(bf16x8, __builtin_shufflevector(lo, hi, 0, 1, 2, 3, 4, 5, 6, 7));
}
template <int NT>
DI void glds16(const void* gbase, unsigned voff, unsigned ldst) {
  unsigned keep;
  if constexpr (NT)
    asm volatile("s_mov_b32 %0, m0\n\ts_mov_b32 m0, %2\n\ts_nop 0\n\tglobal_load_lds_dwordx4 %1, %3 nt\n\ts_mov_b32 m0, %0"
                 : "=&s"(keep) : "v"(voff), "s"(ldst), "s"(gbase) : "memory");
  else
    asm volatile("s_mov_b32 %0, m0\n\ts_mov_b32 m0, %2\n\ts_nop 0\n\tglobal_load_lds_dwordx4 %1, %3\n\ts_mov_b32 m0, %0"
                 : "=&s"(keep) : "v"(voff), "s"(ldst), "s"(gbase) : "memory");
}
DI unsigned lds_addr(const void* p) { return (unsigned)(size_t)(__attribute__((address_space(3))) const char*)p; }

constexpr int K1_STAGE = 49152;
constexpr int K1_LDS = 163840;

__global__ __launch_bounds__(512, 2) void k1_qproj_attn(
    const uint16_t* __restrict__ xA, const uint16_t* __restrict__ kA, const uint16_t* __restrict__ vA,
    const uint16_t* __restrict__ wqA, const uint16_t* __restrict__ xB, const uint16_t* __restrict__ kB,
    const uint16_t* __restrict__ vB, const uint16_t* __restrict__ wqB, uint16_t* __restrict__ ows,
    const uint16_t* __restrict__ woA, const uint16_t* __restrict__ woB, unsigned* __restrict__ cnt) {
  extern __shared__ __attribute__((aligned(16))) char smem[];
  const int tid = threadIdx.x, lane = tid & 63;
  const int w = __builtin_amdgcn_readfirstlane(tid >> 6);
  const int bid = blockIdx.x, xcd = bid & 7, idx = bid >> 3;
  const int X = xcd >> 2, h = (xcd & 3) * 8 + (idx & 7), bg = idx >> 3;
  const int c = w & 3;
  if (tid == 0) cnt[bid * 16] = 0u;
  const int r = lane & 31, hh = lane >> 5, blk = (lane >> 4) & 1, q = (lane & 15) >> 2, p = lane & 3;
  const int troff = (8 * hh + q) * 256 + 32 * blk + 16 * (p & 1) + 8 * (p >> 1);
  int tro[4];
#pragma unroll
  for (int t = 0; t < 4; ++t) tro[t] = troff + ((t ^ q) << 6);
  bf16x8 qf[8];
  unsigned pfk0 = 0, pfk1 = 0;

  if (w < 4) {
    f32x16 acc[4][2];
#pragma unroll
    for (int t = 0; t < 4; ++t)
#pragma unroll
      for (int u = 0; u < 2; ++u)
#pragma unroll
        for (int i = 0; i < 16; ++i) acc[t][u][i] = 0.f;
    const int f = (r >> 1) & 7;
    int xo[4];
#pragma unroll
    for (int s = 0; s < 4; ++s) xo[s] = 16384 + c * 8192 + r * 128 + (((2 * s + hh) ^ f) << 4);
    bf16x8 af[2][4], bf[2][2];
    auto rdA = [&](const char* lb, int s, int slot, int t) {
      af[slot][t] = tr_frag(lb + tro[t] + s * 4096, lb + tro[t] + s * 4096 + 1024);
    };
    auto rdB = [&](const char* lb, int s, int slot) {
      bf[slot][0] = *(const bf16x8*)(lb + xo[s]);
      bf[slot][1] = *(const bf16x8*)(lb + xo[s] + 4096);
    };
    auto kstep = [&](int slot, const char* lb, int s) {
      const int ns = slot ^ 1;
      acc[0][0] = MFMA32(af[slot][0], bf[slot][0], acc[0][0]); SCHED0();
      rdB(lb, s, ns); SCHED0();
      acc[0][1] = MFMA32(af[slot][0], bf[slot][1], acc[0][1]); SCHED0();
      rdA(lb, s, ns, 0); SCHED0();
      acc[1][0] = MFMA32(af[slot][1], bf[slot][0], acc[1][0]); SCHED0();
      rdA(lb, s, ns, 1); SCHED0();
      acc[1][1] = MFMA32(af[slot][1], bf[slot][1], acc[1][1]); SCHED0();
      rdA(lb, s, ns, 2); SCHED0();
      acc[2][0] = MFMA32(af[slot][2], bf[slot][0], acc[2][0]); SCHED0();
      rdA(lb, s, ns, 3); SCHED0();
      acc[2][1] = MFMA32(af[slot][2], bf[slot][1], acc[2][1]);
      acc[3][0] = MFMA32(af[slot][3], bf[slot][0], acc[3][0]);
      acc[3][1] = MFMA32(af[slot][3], bf[slot][1], acc[3][1]); SCHED0();
    };
    __builtin_amdgcn_s_barrier();
    MEMFENCE();
    rdB(smem, 0, 0);
#pragma unroll
    for (int t = 0; t < 4; ++t) rdA(smem, 0, 0, t);
    SCHED0();
    unsigned cur = 0;
#pragma unroll 1
    for (int st = 0; st < 8; ++st) {
      const unsigned nxt = (cur == 2 * K1_STAGE) ? 0u : cur + K1_STAGE;
      const char* lb = smem + cur;
      kstep(0, lb, 1);
      kstep(1, lb, 2);
      kstep(0, lb, 3);
      asm volatile("s_waitcnt lgkmcnt(0)" ::: "memory");
      __builtin_amdgcn_s_barrier();
      MEMFENCE();
      kstep(1, smem + nxt, 0);
      cur = nxt;
    }
    u32x4* qd = (u32x4*)(smem + 65536 + c * 8192) + lane;
#pragma unroll
    for (int t = 0; t < 4; ++t)
#pragma unroll
      for (int s = 0; s < 2; ++s) {
        qd[(2 * t + s) * 64] = __builtin_bit_cast(u32x4, pack8(acc[t][1], s));
        qf[2 * t + s] = pack8(acc[t][0], s);
      }
    asm volatile("s_waitcnt lgkmcnt(0)" ::: "memory");
    __builtin_amdgcn_s_barrier();
    MEMFENCE();
  } else {
    const uint16_t* x = X ? xB : xA;
    const uint16_t* kc = X ? kB : kA;
    const uint16_t* vc = X ? vB : vA;
    const uint16_t* wq = X ? wqB : wqA;
    const char* xb = (const char*)(x + (size_t)((bg * 8 + c * 2) * 32) * 512);
    const char* wb = (const char*)(wq + (size_t)(c * 16) * 4096 + h * 128);
    const unsigned xoff0 = (lane >> 3) * 1024 + (((lane & 7) ^ (lane >> 4)) << 4);
    const unsigned xoff1 = (8 + (lane >> 3)) * 1024 + (((lane & 7) ^ ((lane >> 4) | 4)) << 4);
    const unsigned woff = (lane >> 4) * 8192 + (((lane & 15) ^ ((lane >> 4) << 2)) << 4);
    const unsigned sbase = lds_addr(smem);
    auto stage = [&](int st, unsigned bufoff) {
      const unsigned lb = sbase + bufoff;
      const char* wg = wb + (size_t)st * (64 * 8192);
#pragma unroll
      for (int j = 0; j < 4; ++j) glds16<1>(wg + j * (4 * 8192), woff, lb + c * 4096 + j * 1024);
      const unsigned lx = lb + 16384 + c * 8192;
      const char* xg = xb + st * 128;
#pragma unroll
      for (int j = 0; j < 4; ++j) {
        glds16<0>(xg + j * 16384, xoff0, lx + j * 2048);
        glds16<0>(xg + j * 16384, xoff1, lx + j * 2048 + 1024);
      }
    };
    const unsigned kvo = lane * 16;
    u32x4 kv[2][16];
#define KVNT " nt"
    auto kvchunk = [&](int ch) {
      const int isv = ch >> 2, u = (ch >> 1) & 1, o = isv * 8 + (ch & 1) * 4;
      const uint16_t* g = (isv ? vc : kc) + (size_t)((bg * 8 + c * 2 + u) * 32 + h) * 4096 + (ch & 1) * 2048;
      asm volatile(
          "global_load_dwordx4 %0, %4, %5" KVNT "\n\t"
          "global_load_dwordx4 %1, %4, %5 offset:1024" KVNT "\n\t"
          "global_load_dwordx4 %2, %4, %5 offset:2048" KVNT "\n\t"
          "global_load_dwordx4 %3, %4, %5 offset:3072" KVNT
          : "=v"(kv[u][o]), "=v"(kv[u][o + 1]), "=v"(kv[u][o + 2]), "=v"(kv[u][o + 3])
          : "v"(kvo), "s"(g) : "memory");
    };
#define KV_PIN(u, o, N) asm volatile("s_waitcnt vmcnt(%8)" : "+v"(kv[u][o]), "+v"(kv[u][o + 1]), "+v"(kv[u][o + 2]), "+v"(kv[u][o + 3]), \
                                     "+v"(kv[u][o + 4]), "+v"(kv[u][o + 5]), "+v"(kv[u][o + 6]), "+v"(kv[u][o + 7]) : "i"(N) : "memory")
    auto putK = [&](int u) {
      char* kl = smem + 98304 + (c * 2 + u) * 8192 + (lane >> 4) * 256;
#pragma unroll
      for (int i = 0; i < 8; ++i) *(u32x4*)(kl + i * 1024 + (((lane & 15) ^ ((4 * i + (lane >> 4)) & 15)) << 4)) = kv[u][i];
    };
    auto putV = [&](int u) {
      char* vl = smem + u * 32768 + c * 8192 + (lane >> 4) * 256 + (((lane & 15) ^ ((lane >> 4) << 2)) << 4);
#pragma unroll
      for (int i = 0; i < 8; ++i) *(u32x4*)(vl + i * 1024) = kv[u][8 + i];
    };
    stage(0, 0);
    stage(1, K1_STAGE);
    stage(2, 2 * K1_STAGE);
    kvchunk(0);
    kvchunk(1);
    asm volatile("s_waitcnt vmcnt(32)" ::: "memory");
    __builtin_amdgcn_s_barrier();
    MEMFENCE();
    unsigned pf0 = 0, pf1 = 0;
#pragma unroll
    for (int st = 0; st < 8; ++st) {
      if (st == 1) asm volatile("s_waitcnt vmcnt(24)" ::: "memory");
      else if (st < 6) asm volatile("s_waitcnt vmcnt(20)" ::: "memory");
      else if (st == 6) asm volatile("s_waitcnt vmcnt(8)" ::: "memory");
      __builtin_amdgcn_s_barrier();
      MEMFENCE();
      if (st < 5) stage(st + 3, (st % 3) * K1_STAGE);
      if (st < 6) kvchunk(st + 2);
      if (st == 5) {
        KV_PIN(0, 0, 24); KV_PIN(1, 0, 24);
        putK(0); putK(1);
      }
      if (st == 6) {
        const char* pw = (const char*)(X ? woB : woA) + (size_t)((xcd & 3) * 32 + idx) * 32768 + c * 8192;
        asm volatile("global_load_dword %0, %2, %3\n\tglobal_load_dword %1, %2, %3 offset:64"
                     : "=v"(pf0), "=v"(pf1) : "v"(lane * 128), "s"(pw) : "memory");
        KV_PIN(0, 8, 10);
        putV(0);
      }
    }
    KV_PIN(1, 8, 2);
    putV(1);
    pfk0 = pf0; pfk1 = pf1;
    asm volatile("s_waitcnt lgkmcnt(0)" ::: "memory");
    __builtin_amdgcn_s_barrier();
    MEMFENCE();
    const u32x4* qs = (const u32x4*)(smem + 65536 + c * 8192) + lane;
#pragma unroll
    for (int i = 0; i < 8; ++i) qf[i] = __builtin_bit_cast(bf16x8, qs[i * 64]);
  }

  const int unit = c * 2 + (w >> 2), b = bg * 8 + unit;
  const char* kl = smem + 98304 + unit * 8192;
  const char* vl = smem + (w >> 2) * 32768 + c * 8192;
  const int pr = (r & 0x13) | ((r & 4) << 1) | ((r & 8) >> 1);
  f32x16 sacc, sac1;
#pragma unroll
  for (int i = 0; i < 16; ++i) { sacc[i] = 0.f; sac1[i] = 0.f; }
#pragma unroll
  for (int i = 0; i < 8; i += 2) {
    sacc = MFMA32(*(const bf16x8*)(kl + pr * 256 + (((2 * i + hh) ^ (pr & 15)) << 4)), qf[i], sacc);
    sac1 = MFMA32(*(const bf16x8*)(kl + pr * 256 + (((2 * i + 2 + hh) ^ (pr & 15)) << 4)), qf[i + 1], sac1);
  }
  sacc += sac1;

  const float scale = 0.08838834764831845f;
  float m = -3.0e38f;
#pragma unroll
  for (int i = 0; i < 16; i += 2) {
    unsigned u = cvtpk(sacc[i], sacc[i + 1]);
    float a = __uint_as_float(u << 16) * scale, cc = __uint_as_float(u & 0xffff0000u) * scale;
    unsigned u2 = cvtpk(a, cc);
    sacc[i] = __uint_as_float(u2 << 16);
    sacc[i + 1] = __uint_as_float(u2 & 0xffff0000u);
    m = fmaxf(m, fmaxf(sacc[i], sacc[i + 1]));
  }
  {
    auto rr = __builtin_amdgcn_permlane32_swap(__float_as_uint(m), __float_as_uint(m), false, false);
    m = fmaxf(__uint_as_float(rr[0]), __uint_as_float(rr[1]));
  }
  float sum = 0.f;
#pragma unroll
  for (int i = 0; i < 16; ++i) {
    sacc[i] = __builtin_amdgcn_exp2f((sacc[i] - m) * 1.4426950408889634f);
    sum += sacc[i];
  }
  {
    auto rr = __builtin_amdgcn_permlane32_swap(__float_as_uint(sum), __float_as_uint(sum), false, false);
    sum = __uint_as_float(rr[0]) + __uint_as_float(rr[1]);
  }
  const float inv = 1.0f / sum;
#pragma unroll
  for (int i = 0; i < 16; ++i) sacc[i] *= inv;
  bf16x8 pf0 = pack8(sacc, 0), pf1 = pack8(sacc, 1);

  u32x4* od = (u32x4*)(ows + ((size_t)((X * 32 + b) * 256 + h * 8) * 64 + lane) * 8);
#pragma unroll
  for (int t = 0; t < 4; ++t) {
    f32x16 o;
#pragma unroll
    for (int i = 0; i < 16; ++i) o[i] = 0.f;
    const char* pa = vl + tro[t];
    o = MFMA32(tr_frag(pa, pa + 1024), pf0, o);
    o = MFMA32(tr_frag(pa + 4096, pa + 4096 + 1024), pf1, o);
    {
      u32x4 v0 = __builtin_bit_cast(u32x4, pack8(o, 0)), v1 = __builtin_bit_cast(u32x4, pack8(o, 1));
      asm volatile("global_store_dwordx4 %0, %1, off sc1\n\tglobal_store_dwordx4 %0, %2, off offset:1024 sc1\n\ts_nop 1"
                   ::"v"(od + (2 * t) * 64), "v"(v0), "v"(v1) : "memory");
    }
  }
  asm volatile("s_waitcnt vmcnt(0)" : "+v"(pfk0), "+v"(pfk1)::"memory");
}

constexpr int K2_NB = 8;
constexpr int K2_LDS = K2_NB * 16384;
constexpr int K2_OD = 16;

__global__ __launch_bounds__(512, 2) void k2_oproj(const uint16_t* __restrict__ woA, const uint16_t* __restrict__ woB,
                                                   const uint16_t* __restrict__ ows, float* __restrict__ slab,
                                                   unsigned* __restrict__ cnt, uint16_t* __restrict__ out) {
  extern __shared__ __attribute__((aligned(16))) char smem[];
  const int tid = threadIdx.x, lane = tid & 63;
  const int w = __builtin_amdgcn_readfirstlane(tid >> 6);
  const int bid = blockIdx.x, xcd = bid & 7, idx = bid >> 3;
  const int X = xcd >> 2, hg = xcd & 3, rt = idx >> 2, ct = idx & 3;
  const int c = w & 3;
  const int tile = (X * 8 + rt) * 4 + ct;

  if (w < 4) {
    const int hh = lane >> 5, blk = (lane >> 4) & 1, q = (lane & 15) >> 2, p = lane & 3;
    const int troff = (8 * hh + q) * 256 + 32 * blk + 16 * (p & 1) + 8 * (p >> 1);
    unsigned pa[2][4];
#pragma unroll
    for (int t = 0; t < 4; ++t) {
      pa[0][t] = lds_addr(smem) + troff + ((t ^ q) << 6);
      pa[1][t] = pa[0][t] + 65536;
      asm volatile("" : "+v"(pa[0][t]), "+v"(pa[1][t]));
    }
    const char* ob = (const char*)ows + (size_t)((X * 32 + rt * 4 + c) * 256 + hg * 64) * 1024;
    const unsigned oo = lane * 16;
    u32x4 of[K2_OD];
    f32x16 acc[4];
#pragma unroll
    for (int t = 0; t < 4; ++t)
#pragma unroll
      for (int i = 0; i < 16; ++i) acc[t][i] = 0.f;
    bf16x8 af[2][4];
    auto oload = [&](int ks) {
      asm volatile("global_load_dwordx4 %0, %1, %2" : "=v"(of[ks % K2_OD]) : "v"(oo), "s"(ob + ks * 1024) : "memory");
    };
    auto rdA = [&](int ks, int t) {
      const int o = ((ks >> 2) % K2_NB) * 16384 + (ks & 3) * 4096;
      const unsigned a = pa[o >> 16][t] + (o & 65535);
      s16x4 lo = __builtin_amdgcn_ds_read_tr16_b64_v4i16((lds_s16x4_ptr)(size_t)a);
      s16x4 hi = __builtin_amdgcn_ds_read_tr16_b64_v4i16((lds_s16x4_ptr)(size_t)(a + 1024));
      af[ks & 1][t] = __builtin_bit_cast(bf16x8, __builtin_shufflevector(lo, hi, 0, 1, 2, 3, 4, 5, 6, 7));
    };
#pragma unroll
    for (int ks = 0; ks < K2_OD; ++ks) oload(ks);
    __builtin_amdgcn_s_barrier();
    MEMFENCE();
#pragma unroll
    for (int t = 0; t < 4; ++t) rdA(0, t);
    SCHED0();
#pragma unroll
    for (int ks = 0; ks < 64; ++ks) {
      if ((ks & 3) == 3 && ks < 63) {
        asm volatile("s_waitcnt lgkmcnt(0)" ::: "memory");
        __builtin_amdgcn_s_barrier();
        MEMFENCE();
      }
      {
        constexpr int dummy = 0; (void)dummy;
        const int n = (63 - ks < K2_OD - 1) ? 63 - ks : K2_OD - 1;
        asm volatile("s_waitcnt vmcnt(%1)" : "+v"(of[ks % K2_OD]) : "i"(n) : "memory");
      }
      const bf16x8 bfr = __builtin_bit_cast(bf16x8, of[ks % K2_OD]);
      acc[0] = MFMA32(af[ks & 1][0], bfr, acc[0]); SCHED0();
      if (ks < 63) { rdA(ks + 1, 0); rdA(ks + 1, 1); } SCHED0();
      acc[1] = MFMA32(af[ks & 1][1], bfr, acc[1]); SCHED0();
      if (ks < 63) { rdA(ks + 1, 2); rdA(ks + 1, 3); } SCHED0();
      acc[2] = MFMA32(af[ks & 1][2], bfr, acc[2]);
      acc[3] = MFMA32(af[ks & 1][3], bfr, acc[3]); SCHED0();
      if (ks + K2_OD < 64) oload(ks + K2_OD);
      SCHED0();
    }
    if (c == hg) {
      f32x4* sd = (f32x4*)smem + lane;
#pragma unroll
      for (int t = 0; t < 4; ++t)
#pragma unroll
        for (int g = 0; g < 4; ++g) sd[(t * 4 + g) * 64] = f32x4{acc[t][4 * g], acc[t][4 * g + 1], acc[t][4 * g + 2], acc[t][4 * g + 3]};
      asm volatile("s_waitcnt lgkmcnt(0)" ::: "memory");
    } else {
      const float* dst = slab + ((size_t)(tile * 4 + c) * 3 + (hg < c ? hg : hg - 1)) * 4096;
      const unsigned so = lane * 16;
      asm volatile("s_nop 7\n\ts_nop 7" ::: "memory");
#pragma unroll
      for (int t = 0; t < 4; ++t) {
        f32x4 v0 = {acc[t][0], acc[t][1], acc[t][2], acc[t][3]}, v1 = {acc[t][4], acc[t][5], acc[t][6], acc[t][7]};
        f32x4 v2 = {acc[t][8], acc[t][9], acc[t][10], acc[t][11]}, v3 = {acc[t][12], acc[t][13], acc[t][14], acc[t][15]};
        asm volatile("global_store_dwordx4 %0, %2, %1 sc1\n\t"
                     "global_store_dwordx4 %0, %3, %1 offset:1024 sc1\n\t"
                     "global_store_dwordx4 %0, %4, %1 offset:2048 sc1\n\t"
                     "global_store_dwordx4 %0, %5, %1 offset:3072 sc1\n\ts_nop 1"
                     ::"v"(so), "s"(dst + t * 1024), "v"(v0), "v"(v1), "v"(v2), "v"(v3) : "memory");
      }
      asm volatile("s_waitcnt vmcnt(0)" ::: "memory");
      if (lane == 0) __hip_atomic_fetch_add(cnt + (tile * 4 + c) * 16, 1u, __ATOMIC_RELAXED, __HIP_MEMORY_SCOPE_AGENT);
    }
  } else {
    const uint16_t* wo = X ? woB : woA;
    const char* wb = (const char*)(wo + (size_t)(hg * 1024 + c * 16) * 512 + ct * 128);
    const unsigned woff = (lane >> 4) * 1024 + (((lane & 15) ^ ((lane >> 4) << 2)) << 4);
    const unsigned sbase = lds_addr(smem) + c * 4096;
    auto stage = [&](int st) {
#pragma unroll
      for (int j = 0; j < 4; ++j) glds16<0>(wb + (size_t)st * 65536 + j * 4096, woff, sbase + (st % K2_NB) * 16384 + j * 1024);
    };
#pragma unroll
    for (int st = 0; st < K2_NB; ++st) stage(st);
    asm volatile("s_waitcnt vmcnt(%0)" ::"i"(4 * (K2_NB - 1)) : "memory");
    __builtin_amdgcn_s_barrier();
    MEMFENCE();
#pragma unroll
    for (int st = 0; st < 15; ++st) {
      constexpr int dummy = 0; (void)dummy;
      const int last = (st + K2_NB - 1 < 15) ? st + K2_NB - 1 : 15;
      asm volatile("s_waitcnt vmcnt(%0)" ::"i"(4 * (last - (st + 1))) : "memory");
      __builtin_amdgcn_s_barrier();
      MEMFENCE();
      if (st + K2_NB < 16) stage(st + K2_NB);
    }
    if (w == 4) {
      const unsigned* cp = cnt + (tile * 4 + hg) * 16;
      for (int spin = 0; spin < (1 << 20); ++spin) {
        if (__hip_atomic_load(cp, __ATOMIC_RELAXED, __HIP_MEMORY_SCOPE_AGENT) >= 3u) break;
        __builtin_amdgcn_s_sleep(2);
      }
    }
  }
  MEMFENCE();
  __builtin_amdgcn_s_barrier();
  MEMFENCE();
  {
    const float* src = slab + (size_t)(tile * 4 + hg) * 3 * 4096 + w * 512;
    const unsigned so = lane * 16;
    f32x4 a0, a1, b0, b1, c0, c1;
    asm volatile("global_load_dwordx4 %0, %6, %7 sc1\n\t"
                 "global_load_dwordx4 %1, %6, %7 offset:1024 sc1\n\t"
                 "global_load_dwordx4 %2, %6, %8 sc1\n\t"
                 "global_load_dwordx4 %3, %6, %8 offset:1024 sc1\n\t"
                 "global_load_dwordx4 %4, %6, %9 sc1\n\t"
                 "global_load_dwordx4 %5, %6, %9 offset:1024 sc1\n\t"
                 "s_waitcnt vmcnt(0)"
                 : "=&v"(a0), "=&v"(a1), "=&v"(b0), "=&v"(b1), "=&v"(c0), "=&v"(c1)
                 : "v"(so), "s"(src), "s"(src + 4096), "s"(src + 8192) : "memory");
    const f32x4* own = (const f32x4*)smem + (w * 2) * 64 + lane;
    const f32x4 o0 = own[0], o1 = own[64];
    f32x4 s0, s1;
    if (hg == 0) { s0 = ((o0 + a0) + b0) + c0; s1 = ((o1 + a1) + b1) + c1; }
    else if (hg == 1) { s0 = ((a0 + o0) + b0) + c0; s1 = ((a1 + o1) + b1) + c1; }
    else if (hg == 2) { s0 = ((a0 + b0) + o0) + c0; s1 = ((a1 + b1) + o1) + c1; }
    else { s0 = ((a0 + b0) + c0) + o0; s1 = ((a1 + b1) + c1) + o1; }
    u32x4 o = {cvtpk(s0[0], s0[1]), cvtpk(s0[2], s0[3]), cvtpk(s1[0], s1[1]), cvtpk(s1[2], s1[3])};
    uint16_t* dst = out + (size_t)((rt * 4 + hg) * 32 + (lane & 31)) * 1024 + X * 512 + ct * 128 + (w >> 1) * 32 + (w & 1) * 16 + (lane >> 5) * 8;
    *(u32x4*)dst = o;
  }
}

extern "C" void kernel_launch(void* const* d_in, const int* in_sizes, int n_in, void* d_out, int out_size,
                              void* d_ws, size_t ws_size, hipStream_t stream) {
  (void)in_sizes; (void)n_in; (void)out_size; (void)ws_size;
  (void)hipFuncSetAttribute((const void*)k1_qproj_attn, hipFuncAttributeMaxDynamicSharedMemorySize, K1_LDS);
  (void)hipFuncSetAttribute((const void*)k2_oproj, hipFuncAttributeMaxDynamicSharedMemorySize, K2_LDS);
  const uint16_t* xA = (const uint16_t*)d_in[0];
  const uint16_t* kA = (const uint16_t*)d_in[1];
  const uint16_t* vA = (const uint16_t*)d_in[2];
  const uint16_t* wqA = (const uint16_t*)d_in[3];
  const uint16_t* woA = (const uint16_t*)d_in[4];
  const uint16_t* xB = (const uint16_t*)d_in[5];
  const uint16_t* kB = (const uint16_t*)d_in[6];
  const uint16_t* vB = (const uint16_t*)d_in[7];
  const uint16_t* wqB = (const uint16_t*)d_in[8];
  const uint16_t* woB = (const uint16_t*)d_in[9];
  uint16_t* ows = (uint16_t*)d_ws;
  float* slab = (float*)((char*)d_ws + (16u << 20));
  unsigned* cnt = (unsigned*)((char*)d_ws + (32u << 20));
  k1_qproj_attn<<<256, 512, K1_LDS, stream>>>(xA, kA, vA, wqA, xB, kB, vB, wqB, ows, woA, woB, cnt);
  k2_oproj<<<256, 512, K2_LDS, stream>>>(woA, woB, ows, slab, cnt, (uint16_t*)d_out);
}
```

```cpp
#include <hip/hip_runtime.h>
#include <hip/hip_bf16.h>
#include <stdint.h>

typedef __attribute__((ext_vector_type(8))) __bf16 bf16x8;
typedef __attribute__((ext_vector_type(2))) __bf16 bf16x2;
typedef __attribute__((ext_vector_type(4))) short s16x4;
typedef __attribute__((ext_vector_type(2))) float f32x2;
typedef __attribute__((ext_vector_type(4))) float f32x4;
typedef __attribute__((ext_vector_type(16))) float f32x16;
typedef __attribute__((ext_vector_type(4))) unsigned u32x4;
typedef __attribute__((address_space(3))) s16x4* lds_s16x4_ptr;

#define DI __device__ __forceinline__
#define MFMA32(a, b, c) __builtin_amdgcn_mfma_f32_32x32x16_bf16((a), (b), (c), 0, 0, 0)
#define MEMFENCE() asm volatile("" ::: "memory")
#define SCHED0() __builtin_amdgcn_sched_barrier(0)

DI unsigned cvtpk(float lo, float hi) {
  f32x2 v = {lo, hi};
  bf16x2 r = __builtin_convertvector(v, bf16x2);
  return __builtin_bit_cast(unsigned, r);
}
DI bf16x8 pack8(const f32x16& x, int s) {
  u32x4 p = {cvtpk(x[8 * s + 0], x[8 * s + 1]), cvtpk(x[8 * s + 2], x[8 * s + 3]),
             cvtpk(x[8 * s + 4], x[8 * s + 5]), cvtpk(x[8 * s + 6], x[8 * s + 7])};
  return __builtin_bit_cast(bf16x8, p);
}
DI s16x4 tr_read(const char* p) { return __builtin_amdgcn_ds_read_tr16_b64_v4i16((lds_s16x4_ptr)(p)); }
DI bf16x8 tr_frag(const char* p0, const char* p1) {
  s16x4 lo = tr_read(p0), hi = tr_read(p1);
  return __builtin_bit_cast(bf16x8, __builtin_shufflevector(lo, hi, 0, 1, 2, 3, 4, 5, 6, 7));
}
template <int NT>
DI void glds16(const void* gbase, unsigned voff, unsigned ldst) {
  unsigned keep;
  if constexpr (NT)
    asm volatile("s_mov_b32 %0, m0\n\ts_mov_b32 m0, %2\n\ts_nop 0\n\tglobal_load_lds_dwordx4 %1, %3 nt\n\ts_mov_b32 m0, %0"
                 : "=&s"(keep) : "v"(voff), "s"(ldst), "s"(gbase) : "memory");
  else
    asm volatile("s_mov_b32 %0, m0\n\ts_mov_b32 m0, %2\n\ts_nop 0\n\tglobal_load_lds_dwordx4 %1, %3\n\ts_mov_b32 m0, %0"
                 : "=&s"(keep) : "v"(voff), "s"(ldst), "s"(gbase) : "memory");
}
DI unsigned lds_addr(const void* p) { return (unsigned)(size_t)(__attribute__((address_space(3))) const char*)p; }

constexpr int K1_STAGE = 49152;
constexpr int K1_LDS = 163840;

__global__ __launch_bounds__(512, 2) void k1_qproj_attn(
    const uint16_t* __restrict__ xA, const uint16_t* __restrict__ kA, const uint16_t* __restrict__ vA,
    const uint16_t* __restrict__ wqA, const uint16_t* __restrict__ xB, const uint16_t* __restrict__ kB,
    const uint16_t* __restrict__ vB, const uint16_t* __restrict__ wqB, uint16_t* __restrict__ ows,
    const uint16_t* __restrict__ woA, const uint16_t* __restrict__ woB, unsigned* __restrict__ cnt) {
  extern __shared__ __attribute__((aligned(16))) char smem[];
  const int tid = threadIdx.x, lane = tid & 63;
  const int w = __builtin_amdgcn_readfirstlane(tid >> 6);
  const int bid = blockIdx.x, xcd = bid & 7, idx = bid >> 3;
  const int X = xcd >> 2, h = (xcd & 3) * 8 + (idx & 7), bg = idx >> 3;
  const int c = w & 3;
  if (tid == 0) cnt[bid * 16] = 0u;
  const int r = lane & 31, hh = lane >> 5, blk = (lane >> 4) & 1, q = (lane & 15) >> 2, p = lane & 3;
  const int troff = (8 * hh + q) * 256 + 32 * blk + 16 * (p & 1) + 8 * (p >> 1);
  int tro[4];
#pragma unroll
  for (int t = 0; t < 4; ++t) tro[t] = troff + ((t ^ q) << 6);
  bf16x8 qf[8];
  unsigned pfk0 = 0, pfk1 = 0;

  if (w < 4) {
    f32x16 acc[4][2];
#pragma unroll
    for (int t = 0; t < 4; ++t)
#pragma unroll
      for (int u = 0; u < 2; ++u)
#pragma unroll
        for (int i = 0; i < 16; ++i) acc[t][u][i] = 0.f;
    const int f = (r >> 1) & 7;
    int xo[4];
#pragma unroll
    for (int s = 0; s < 4; ++s) xo[s] = 16384 + c * 8192 + r * 128 + (((2 * s + hh) ^ f) << 4);
    bf16x8 af[2][4], bf[2][2];
    auto rdA = [&](const char* lb, int s, int slot, int t) {
      af[slot][t] = tr_frag(lb + tro[t] + s * 4096, lb + tro[t] + s * 4096 + 1024);
    };
    auto rdB = [&](const char* lb, int s, int slot) {
      bf[slot][0] = *(const bf16x8*)(lb + xo[s]);
      bf[slot][1] = *(const bf16x8*)(lb + xo[s] + 4096);
    };
    auto kstep = [&](int slot, const char* lb, int s) {
      const int ns = slot ^ 1;
      acc[0][0] = MFMA32(af[slot][0], bf[slot][0], acc[0][0]); SCHED0();
      rdB(lb, s, ns); SCHED0();
      acc[0][1] = MFMA32(af[slot][0], bf[slot][1], acc[0][1]); SCHED0();
      rdA(lb, s, ns, 0); SCHED0();
      acc[1][0] = MFMA32(af[slot][1], bf[slot][0], acc[1][0]); SCHED0();
      rdA(lb, s, ns, 1); SCHED0();
      acc[1][1] = MFMA32(af[slot][1], bf[slot][1], acc[1][1]); SCHED0();
      rdA(lb, s, ns, 2); SCHED0();
      acc[2][0] = MFMA32(af[slot][2], bf[slot][0], acc[2][0]); SCHED0();
      rdA(lb, s, ns, 3); SCHED0();
      acc[2][1] = MFMA32(af[slot][2], bf[slot][1], acc[2][1]);
      acc[3][0] = MFMA32(af[slot][3], bf[slot][0], acc[3][0]);
      acc[3][1] = MFMA32(af[slot][3], bf[slot][1], acc[3][1]); SCHED0();
    };
    unsigned tch;
    {
      const int u = bg * 8 + c * 2 + (lane & 1), sel = (lane >> 1) & 3;
      const char* pk = (const char*)((X ? kB : kA) + (size_t)(u * 32 + h) * 4096);
      const char* pv = (const char*)((X ? vB : vA) + (size_t)(u * 32 + h) * 4096);
      const char* po = (const char*)(ows + (size_t)((X * 32 + u) * 256 + h * 8) * 512);
      const char* pt = sel == 0 ? pk : (sel == 1 ? pv : po);
      asm volatile("global_load_dword %0, %1, off nt" : "=v"(tch) : "v"(pt) : "memory");
    }
    __builtin_amdgcn_s_barrier();
    MEMFENCE();
    rdB(smem, 0, 0);
#pragma unroll
    for (int t = 0; t < 4; ++t) rdA(smem, 0, 0, t);
    SCHED0();
    unsigned cur = 0;
#pragma unroll 1
    for (int st = 0; st < 8; ++st) {
      const unsigned nxt = (cur == 2 * K1_STAGE) ? 0u : cur + K1_STAGE;
      const char* lb = smem + cur;
      kstep(0, lb, 1);
      kstep(1, lb, 2);
      kstep(0, lb, 3);
      asm volatile("s_waitcnt lgkmcnt(0)" ::: "memory");
      __builtin_amdgcn_s_barrier();
      MEMFENCE();
      kstep(1, smem + nxt, 0);
      cur = nxt;
    }
    asm volatile("s_waitcnt vmcnt(0)" : "+v"(tch)::"memory");
    u32x4* qd = (u32x4*)(smem + 65536 + c * 8192) + lane;
#pragma unroll
    for (int t = 0; t < 4; ++t)
#pragma unroll
      for (int s = 0; s < 2; ++s) {
        qd[(2 * t + s) * 64] = __builtin_bit_cast(u32x4, pack8(acc[t][1], s));
        qf[2 * t + s] = pack8(acc[t][0], s);
      }
    asm volatile("s_waitcnt lgkmcnt(0)" ::: "memory");
    __builtin_amdgcn_s_barrier();
    MEMFENCE();
  } else {
    const uint16_t* x = X ? xB : xA;
    const uint16_t* kc = X ? kB : kA;
    const uint16_t* vc = X ? vB : vA;
    const uint16_t* wq = X ? wqB : wqA;
    const char* xb = (const char*)(x + (size_t)((bg * 8 + c * 2) * 32) * 512);
    const char* wb = (const char*)(wq + (size_t)(c * 16) * 4096 + h * 128);
    const unsigned xoff0 = (lane >> 3) * 1024 + (((lane & 7) ^ (lane >> 4)) << 4);
    const unsigned xoff1 = (8 + (lane >> 3)) * 1024 + (((lane & 7) ^ ((lane >> 4) | 4)) << 4);
    const unsigned woff = (lane >> 4) * 8192 + (((lane & 15) ^ ((lane >> 4) << 2)) << 4);
    const unsigned sbase = lds_addr(smem);
    auto stage = [&](int st, unsigned bufoff) {
      const unsigned lb = sbase + bufoff;
      const char* wg = wb + (size_t)st * (64 * 8192);
#pragma unroll
      for (int j = 0; j < 4; ++j) glds16<1>(wg + j * (4 * 8192), woff, lb + c * 4096 + j * 1024);
      const unsigned lx = lb + 16384 + c * 8192;
      const char* xg = xb + st * 128;
#pragma unroll
      for (int j = 0; j < 4; ++j) {
        glds16<0>(xg + j * 16384, xoff0, lx + j * 2048);
        glds16<0>(xg + j * 16384, xoff1, lx + j * 2048 + 1024);
      }
    };
    const unsigned kvo = lane * 16;
    u32x4 kv[2][16];
#define KVNT " nt"
    auto kvchunk = [&](int ch) {
      const int isv = ch >> 2, u = (ch >> 1) & 1, o = isv * 8 + (ch & 1) * 4;
      const uint16_t* g = (isv ? vc : kc) + (size_t)((bg * 8 + c * 2 + u) * 32 + h) * 4096 + (ch & 1) * 2048;
      asm volatile(
          "global_load_dwordx4 %0, %4, %5" KVNT "\n\t"
          "global_load_dwordx4 %1, %4, %5 offset:1024" KVNT "\n\t"
          "global_load_dwordx4 %2, %4, %5 offset:2048" KVNT "\n\t"
          "global_load_dwordx4 %3, %4, %5 offset:3072" KVNT
          : "=v"(kv[u][o]), "=v"(kv[u][o + 1]), "=v"(kv[u][o + 2]), "=v"(kv[u][o + 3])
          : "v"(kvo), "s"(g) : "memory");
    };
#define KV_PIN(u, o, N) asm volatile("s_waitcnt vmcnt(%8)" : "+v"(kv[u][o]), "+v"(kv[u][o + 1]), "+v"(kv[u][o + 2]), "+v"(kv[u][o + 3]), \
                                     "+v"(kv[u][o + 4]), "+v"(kv[u][o + 5]), "+v"(kv[u][o + 6]), "+v"(kv[u][o + 7]) : "i"(N) : "memory")
    auto putK = [&](int u) {
      char* kl = smem + 98304 + (c * 2 + u) * 8192 + (lane >> 4) * 256;
#pragma unroll
      for (int i = 0; i < 8; ++i) *(u32x4*)(kl + i * 1024 + (((lane & 15) ^ ((4 * i + (lane >> 4)) & 15)) << 4)) = kv[u][i];
    };
    auto putV = [&](int u) {
      char* vl = smem + u * 32768 + c * 8192 + (lane >> 4) * 256 + (((lane & 15) ^ ((lane >> 4) << 2)) << 4);
#pragma unroll
      for (int i = 0; i < 8; ++i) *(u32x4*)(vl + i * 1024) = kv[u][8 + i];
    };
    stage(0, 0);
    stage(1, K1_STAGE);
    stage(2, 2 * K1_STAGE);
    kvchunk(0);
    kvchunk(1);
    asm volatile("s_waitcnt vmcnt(32)" ::: "memory");
    __builtin_amdgcn_s_barrier();
    MEMFENCE();
    unsigned pf0 = 0, pf1 = 0;
#pragma unroll
    for (int st = 0; st < 8; ++st) {
      if (st == 1) asm volatile("s_waitcnt vmcnt(24)" ::: "memory");
      else if (st < 6) asm volatile("s_waitcnt vmcnt(20)" ::: "memory");
      else if (st == 6) asm volatile("s_waitcnt vmcnt(8)" ::: "memory");
      __builtin_amdgcn_s_barrier();
      MEMFENCE();
      if (st < 5) stage(st + 3, (st % 3) * K1_STAGE);
      if (st < 6) kvchunk(st + 2);
      if (st == 5) {
        KV_PIN(0, 0, 24); KV_PIN(1, 0, 24);
        putK(0); putK(1);
      }
      if (st == 6) {
        const char* pw = (const char*)(X ? woB : woA) + (size_t)((xcd & 3) * 32 + idx) * 32768 + c * 8192;
        asm volatile("global_load_dword %0, %2, %3\n\tglobal_load_dword %1, %2, %3 offset:64"
                     : "=v"(pf0), "=v"(pf1) : "v"(lane * 128), "s"(pw) : "memory");
        KV_PIN(0, 8, 10);
        putV(0);
      }
    }
    KV_PIN(1, 8, 2);
    putV(1);
    pfk0 = pf0; pfk1 = pf1;
    asm volatile("s_waitcnt lgkmcnt(0)" ::: "memory");
    __builtin_amdgcn_s_barrier();
    MEMFENCE();
    const u32x4* qs = (const u32x4*)(smem + 65536 + c * 8192) + lane;
#pragma unroll
    for (int i = 0; i < 8; ++i) qf[i] = __builtin_bit_cast(bf16x8, qs[i * 64]);
  }

  const int unit = c * 2 + (w >> 2), b = bg * 8 + unit;
  const char* kl = smem + 98304 + unit * 8192;
  const char* vl = smem + (w >> 2) * 32768 + c * 8192;
  const int pr = (r & 0x13) | ((r & 4) << 1) | ((r & 8) >> 1);
  f32x16 sacc, sac1;
#pragma unroll
  for (int i = 0; i < 16; ++i) { sacc[i] = 0.f; sac1[i] = 0.f; }
#pragma unroll
  for (int i = 0; i < 8; i += 2) {
    sacc = MFMA32(*(const bf16x8*)(kl + pr * 256 + (((2 * i + hh) ^ (pr & 15)) << 4)), qf[i], sacc);
    sac1 = MFMA32(*(const bf16x8*)(kl + pr * 256 + (((2 * i + 2 + hh) ^ (pr & 15)) << 4)), qf[i + 1], sac1);
  }
  sacc += sac1;

  const float scale = 0.08838834764831845f;
  float m = -3.0e38f;
#pragma unroll
  for (int i = 0; i < 16; i += 2) {
    unsigned u = cvtpk(sacc[i], sacc[i + 1]);
    float a = __uint_as_float(u << 16) * scale, cc = __uint_as_float(u & 0xffff0000u) * scale;
    unsigned u2 = cvtpk(a, cc);
    sacc[i] = __uint_as_float(u2 << 16);
    sacc[i + 1] = __uint_as_float(u2 & 0xffff0000u);
    m = fmaxf(m, fmaxf(sacc[i], sacc[i + 1]));
  }
  {
    auto rr = __builtin_amdgcn_permlane32_swap(__float_as_uint(m), __float_as_uint(m), false, false);
    m = fmaxf(__uint_as_float(rr[0]), __uint_as_float(rr[1]));
  }
  float sum = 0.f;
#pragma unroll
  for (int i = 0; i < 16; ++i) {
    sacc[i] = __builtin_amdgcn_exp2f((sacc[i] - m) * 1.4426950408889634f);
    sum += sacc[i];
  }
  {
    auto rr = __builtin_amdgcn_permlane32_swap(__float_as_uint(sum), __float_as_uint(sum), false, false);
    sum = __uint_as_float(rr[0]) + __uint_as_float(rr[1]);
  }
  const float inv = 1.0f / sum;
#pragma unroll
  for (int i = 0; i < 16; ++i) sacc[i] *= inv;
  bf16x8 pf0 = pack8(sacc, 0), pf1 = pack8(sacc, 1);

  u32x4* od = (u32x4*)(ows + ((size_t)((X * 32 + b) * 256 + h * 8) * 64 + lane) * 8);
#pragma unroll
  for (int t = 0; t < 4; ++t) {
    f32x16 o;
#pragma unroll
    for (int i = 0; i < 16; ++i) o[i] = 0.f;
    const char* pa = vl + tro[t];
    o = MFMA32(tr_frag(pa, pa + 1024), pf0, o);
    o = MFMA32(tr_frag(pa + 4096, pa + 4096 + 1024), pf1, o);
    {
      u32x4 v0 = __builtin_bit_cast(u32x4, pack8(o, 0)), v1 = __builtin_bit_cast(u32x4, pack8(o, 1));
      asm volatile("global_store_dwordx4 %0, %1, off sc1\n\tglobal_store_dwordx4 %0, %2, off offset:1024 sc1\n\ts_nop 1"
                   ::"v"(od + (2 * t) * 64), "v"(v0), "v"(v1) : "memory");
    }
  }
  asm volatile("s_waitcnt vmcnt(0)" : "+v"(pfk0), "+v"(pfk1)::"memory");
}

constexpr int K2_NB = 8;
constexpr int K2_LDS = K2_NB * 16384;
constexpr int K2_OD = 16;

__global__ __launch_bounds__(512, 2) void k2_oproj(const uint16_t* __restrict__ woA, const uint16_t* __restrict__ woB,
                                                   const uint16_t* __restrict__ ows, float* __restrict__ slab,
                                                   unsigned* __restrict__ cnt, uint16_t* __restrict__ out) {
  extern __shared__ __attribute__((aligned(16))) char smem[];
  const int tid = threadIdx.x, lane = tid & 63;
  const int w = __builtin_amdgcn_readfirstlane(tid >> 6);
  const int bid = blockIdx.x, xcd = bid & 7, idx = bid >> 3;
  const int X = xcd >> 2, hg = xcd & 3, rt = idx >> 2, ct = idx & 3;
  const int c = w & 3;
  const int tile = (X * 8 + rt) * 4 + ct;

  if (w < 4) {
    const int hh = lane >> 5, blk = (lane >> 4) & 1, q = (lane & 15) >> 2, p = lane & 3;
    const int troff = (8 * hh + q) * 256 + 32 * blk + 16 * (p & 1) + 8 * (p >> 1);
    unsigned pa[2][4];
#pragma unroll
    for (int t = 0; t < 4; ++t) {
      pa[0][t] = lds_addr(smem) + troff + ((t ^ q) << 6);
      pa[1][t] = pa[0][t] + 65536;
      asm volatile("" : "+v"(pa[0][t]), "+v"(pa[1][t]));
    }
    const char* ob = (const char*)ows + (size_t)((X * 32 + rt * 4 + c) * 256 + hg * 64) * 1024;
    const unsigned oo = lane * 16;
    u32x4 of[K2_OD];
    f32x16 acc[4];
#pragma unroll
    for (int t = 0; t < 4; ++t)
#pragma unroll
      for (int i = 0; i < 16; ++i) acc[t][i] = 0.f;
    bf16x8 af[2][4];
    auto oload = [&](int ks) {
      asm volatile("global_load_dwordx4 %0, %1, %2" : "=v"(of[ks % K2_OD]) : "v"(oo), "s"(ob + ks * 1024) : "memory");
    };
    auto rdA = [&](int ks, int t) {
      const int o = ((ks >> 2) % K2_NB) * 16384 + (ks & 3) * 4096;
      const unsigned a = pa[o >> 16][t] + (o & 65535);
      s16x4 lo = __builtin_amdgcn_ds_read_tr16_b64_v4i16((lds_s16x4_ptr)(size_t)a);
      s16x4 hi = __builtin_amdgcn_ds_read_tr16_b64_v4i16((lds_s16x4_ptr)(size_t)(a + 1024));
      af[ks & 1][t] = __builtin_bit_cast(bf16x8, __builtin_shufflevector(lo, hi, 0, 1, 2, 3, 4, 5, 6, 7));
    };
#pragma unroll
    for (int ks = 0; ks < K2_OD; ++ks) oload(ks);
    __builtin_amdgcn_s_barrier();
    MEMFENCE();
#pragma unroll
    for (int t = 0; t < 4; ++t) rdA(0, t);
    SCHED0();
#pragma unroll
    for (int ks = 0; ks < 64; ++ks) {
      if ((ks & 3) == 3 && ks < 63) {
        asm volatile("s_waitcnt lgkmcnt(0)" ::: "memory");
        __builtin_amdgcn_s_barrier();
        MEMFENCE();
      }
      {
        constexpr int dummy = 0; (void)dummy;
        const int n = (63 - ks < K2_OD - 1) ? 63 - ks : K2_OD - 1;
        asm volatile("s_waitcnt vmcnt(%1)" : "+v"(of[ks % K2_OD]) : "i"(n) : "memory");
      }
      const bf16x8 bfr = __builtin_bit_cast(bf16x8, of[ks % K2_OD]);
      acc[0] = MFMA32(af[ks & 1][0], bfr, acc[0]); SCHED0();
      if (ks < 63) { rdA(ks + 1, 0); rdA(ks + 1, 1); } SCHED0();
      acc[1] = MFMA32(af[ks & 1][1], bfr, acc[1]); SCHED0();
      if (ks < 63) { rdA(ks + 1, 2); rdA(ks + 1, 3); } SCHED0();
      acc[2] = MFMA32(af[ks & 1][2], bfr, acc[2]);
      acc[3] = MFMA32(af[ks & 1][3], bfr, acc[3]); SCHED0();
      if (ks + K2_OD < 64) oload(ks + K2_OD);
      SCHED0();
    }
    if (c == hg) {
      f32x4* sd = (f32x4*)smem + lane;
#pragma unroll
      for (int t = 0; t < 4; ++t)
#pragma unroll
        for (int g = 0; g < 4; ++g) sd[(t * 4 + g) * 64] = f32x4{acc[t][4 * g], acc[t][4 * g + 1], acc[t][4 * g + 2], acc[t][4 * g + 3]};
      asm volatile("s_waitcnt lgkmcnt(0)" ::: "memory");
    } else {
      const float* dst = slab + ((size_t)(tile * 4 + c) * 3 + (hg < c ? hg : hg - 1)) * 4096;
      const unsigned so = lane * 16;
      asm volatile("s_nop 7\n\ts_nop 7" ::: "memory");
#pragma unroll
      for (int t = 0; t < 4; ++t) {
        f32x4 v0 = {acc[t][0], acc[t][1], acc[t][2], acc[t][3]}, v1 = {acc[t][4], acc[t][5], acc[t][6], acc[t][7]};
        f32x4 v2 = {acc[t][8], acc[t][9], acc[t][10], acc[t][11]}, v3 = {acc[t][12], acc[t][13], acc[t][14], acc[t][15]};
        asm volatile("global_store_dwordx4 %0, %2, %1 sc1\n\t"
                     "global_store_dwordx4 %0, %3, %1 offset:1024 sc1\n\t"
                     "global_store_dwordx4 %0, %4, %1 offset:2048 sc1\n\t"
                     "global_store_dwordx4 %0, %5, %1 offset:3072 sc1\n\ts_nop 1"
                     ::"v"(so), "s"(dst + t * 1024), "v"(v0), "v"(v1), "v"(v2), "v"(v3) : "memory");
      }
      asm volatile("s_waitcnt vmcnt(0)" ::: "memory");
      if (lane == 0) __hip_atomic_fetch_add(cnt + (tile * 4 + c) * 16, 1u, __ATOMIC_RELAXED, __HIP_MEMORY_SCOPE_AGENT);
    }
  } else {
    const uint16_t* wo = X ? woB : woA;
    const char* wb = (const char*)(wo + (size_t)(hg * 1024 + c * 16) * 512 + ct * 128);
    const unsigned woff = (lane >> 4) * 1024 + (((lane & 15) ^ ((lane >> 4) << 2)) << 4);
    const unsigned sbase = lds_addr(smem) + c * 4096;
    auto stage = [&](int st) {
#pragma unroll
      for (int j = 0; j < 4; ++j) glds16<0>(wb + (size_t)st * 65536 + j * 4096, woff, sbase + (st % K2_NB) * 16384 + j * 1024);
    };
#pragma unroll
    for (int st = 0; st < K2_NB; ++st) stage(st);
    asm volatile("s_waitcnt vmcnt(%0)" ::"i"(4 * (K2_NB - 1)) : "memory");
    __builtin_amdgcn_s_barrier();
    MEMFENCE();
#pragma unroll
    for (int st = 0; st < 15; ++st) {
      constexpr int dummy = 0; (void)dummy;
      const int last = (st + K2_NB - 1 < 15) ? st + K2_NB - 1 : 15;
      asm volatile("s_waitcnt vmcnt(%0)" ::"i"(4 * (last - (st + 1))) : "memory");
      __builtin_amdgcn_s_barrier();
      MEMFENCE();
      if (st + K2_NB < 16) stage(st + K2_NB);
    }
    if (w == 4) {
      const unsigned* cp = cnt + (tile * 4 + hg) * 16;
      for (int spin = 0; spin < (1 << 20); ++spin) {
        if (__hip_atomic_load(cp, __ATOMIC_RELAXED, __HIP_MEMORY_SCOPE_AGENT) >= 3u) break;
        __builtin_amdgcn_s_sleep(2);
      }
    }
  }
  MEMFENCE();
  __builtin_amdgcn_s_barrier();
  MEMFENCE();
  {
    const float* src = slab + (size_t)(tile * 4 + hg) * 3 * 4096 + w * 512;
    const unsigned so = lane * 16;
    f32x4 a0, a1, b0, b1, c0, c1;
    asm volatile("global_load_dwordx4 %0, %6, %7 sc1\n\t"
                 "global_load_dwordx4 %1, %6, %7 offset:1024 sc1\n\t"
                 "global_load_dwordx4 %2, %6, %8 sc1\n\t"
                 "global_load_dwordx4 %3, %6, %8 offset:1024 sc1\n\t"
                 "global_load_dwordx4 %4, %6, %9 sc1\n\t"
                 "global_load_dwordx4 %5, %6, %9 offset:1024 sc1\n\t"
                 "s_waitcnt vmcnt(0)"
                 : "=&v"(a0), "=&v"(a1), "=&v"(b0), "=&v"(b1), "=&v"(c0), "=&v"(c1)
                 : "v"(so), "s"(src), "s"(src + 4096), "s"(src + 8192) : "memory");
    const f32x4* own = (const f32x4*)smem + (w * 2) * 64 + lane;
    const f32x4 o0 = own[0], o1 = own[64];
    f32x4 s0, s1;
    if (hg == 0) { s0 = ((o0 + a0) + b0) + c0; s1 = ((o1 + a1) + b1) + c1; }
    else if (hg == 1) { s0 = ((a0 + o0) + b0) + c0; s1 = ((a1 + o1) + b1) + c1; }
    else if (hg == 2) { s0 = ((a0 + b0) + o0) + c0; s1 = ((a1 + b1) + o1) + c1; }
    else { s0 = ((a0 + b0) + c0) + o0; s1 = ((a1 + b1) + c1) + o1; }
    u32x4 o = {cvtpk(s0[0], s0[1]), cvtpk(s0[2], s0[3]), cvtpk(s1[0], s1[1]), cvtpk(s1[2], s1[3])};
    uint16_t* dst = out + (size_t)((rt * 4 + hg) * 32 + (lane & 31)) * 1024 + X * 512 + ct * 128 + (w >> 1) * 32 + (w & 1) * 16 + (lane >> 5) * 8;
    *(u32x4*)dst = o;
  }
}

extern "C" void kernel_launch(void* const* d_in, const int* in_sizes, int n_in, void* d_out, int out_size,
                              void* d_ws, size_t ws_size, hipStream_t stream) {
  (void)in_sizes; (void)n_in; (void)out_size; (void)ws_size;
  (void)hipFuncSetAttribute((const void*)k1_qproj_attn, hipFuncAttributeMaxDynamicSharedMemorySize, K1_LDS);
  (void)hipFuncSetAttribute((const void*)k2_oproj, hipFuncAttributeMaxDynamicSharedMemorySize, K2_LDS);
  const uint16_t* xA = (const uint16_t*)d_in[0];
  const uint16_t* kA = (const uint16_t*)d_in[1];
  const uint16_t* vA = (const uint16_t*)d_in[2];
  const uint16_t* wqA = (const uint16_t*)d_in[3];
  const uint16_t* woA = (const uint16_t*)d_in[4];
  const uint16_t* xB = (const uint16_t*)d_in[5];
  const uint16_t* kB = (const uint16_t*)d_in[6];
  const uint16_t* vB = (const uint16_t*)d_in[7];
  const uint16_t* wqB = (const uint16_t*)d_in[8];
  const uint16_t* woB = (const uint16_t*)d_in[9];
  uint16_t* ows = (uint16_t*)d_ws;
  float* slab = (float*)((char*)d_ws + (16u << 20));
  unsigned* cnt = (unsigned*)((char*)d_ws + (32u << 20));
  k1_qproj_attn<<<256, 512, K1_LDS, stream>>>(xA, kA, vA, wqA, xB, kB, vB, wqB, ows, woA, woB, cnt);
  k2_oproj<<<256, 512, K2_LDS, stream>>>(woA, woB, ows, slab, cnt, (uint16_t*)d_out);
}
```

```cpp
#include <hip/hip_runtime.h>
#include <hip/hip_bf16.h>
#include <stdint.h>

typedef __attribute__((ext_vector_type(8))) __bf16 bf16x8;
typedef __attribute__((ext_vector_type(2))) __bf16 bf16x2;
typedef __attribute__((ext_vector_type(4))) short s16x4;
typedef __attribute__((ext_vector_type(2))) float f32x2;
typedef __attribute__((ext_vector_type(4))) float f32x4;
typedef __attribute__((ext_vector_type(16))) float f32x16;
typedef __attribute__((ext_vector_type(4))) unsigned u32x4;
typedef __attribute__((address_space(3))) s16x4* lds_s16x4_ptr;

#define DI __device__ __forceinline__
#define MFMA32(a, b, c) __builtin_amdgcn_mfma_f32_32x32x16_bf16((a), (b), (c), 0, 0, 0)
#define MEMFENCE() asm volatile("" ::: "memory")
#define SCHED0() __builtin_amdgcn_sched_barrier(0)

DI unsigned cvtpk(float lo, float hi) {
  f32x2 v = {lo, hi};
  bf16x2 r = __builtin_convertvector(v, bf16x2);
  return __builtin_bit_cast(unsigned, r);
}
DI bf16x8 pack8(const f32x16& x, int s) {
  u32x4 p = {cvtpk(x[8 * s + 0], x[8 * s + 1]), cvtpk(x[8 * s + 2], x[8 * s + 3]),
             cvtpk(x[8 * s + 4], x[8 * s + 5]), cvtpk(x[8 * s + 6], x[8 * s + 7])};
  return __builtin_bit_cast(bf16x8, p);
}
DI s16x4 tr_read(const char* p) { return __builtin_amdgcn_ds_read_tr16_b64_v4i16((lds_s16x4_ptr)(p)); }
DI bf16x8 tr_frag(const char* p0, const char* p1) {
  s16x4 lo = tr_read(p0), hi = tr_read(p1);
  return __builtin_bit_cast(bf16x8, __builtin_shufflevector(lo, hi, 0, 1, 2, 3, 4, 5, 6, 7));
}
template <int NT>
DI void glds16(const void* gbase, unsigned voff, unsigned ldst) {
  unsigned keep;
  if constexpr (NT)
    asm volatile("s_mov_b32 %0, m0\n\ts_mov_b32 m0, %2\n\ts_nop 0\n\tglobal_load_lds_dwordx4 %1, %3 nt\n\ts_mov_b32 m0, %0"
                 : "=&s"(keep) : "v"(voff), "s"(ldst), "s"(gbase) : "memory");
  else
    asm volatile("s_mov_b32 %0, m0\n\ts_mov_b32 m0, %2\n\ts_nop 0\n\tglobal_load_lds_dwordx4 %1, %3\n\ts_mov_b32 m0, %0"
                 : "=&s"(keep) : "v"(voff), "s"(ldst), "s"(gbase) : "memory");
}
DI unsigned lds_addr(const void* p) { return (unsigned)(size_t)(__attribute__((address_space(3))) const char*)p; }

constexpr int SLAB_STRIDE = 4096 + 32;
constexpr int K1_STAGE = 49152;
constexpr int K1_LDS = 163840;

__global__ __launch_bounds__(512, 2) void k1_qproj_attn(
    const uint16_t* __restrict__ xA, const uint16_t* __restrict__ kA, const uint16_t* __restrict__ vA,
    const uint16_t* __restrict__ wqA, const uint16_t* __restrict__ xB, const uint16_t* __restrict__ kB,
    const uint16_t* __restrict__ vB, const uint16_t* __restrict__ wqB, uint16_t* __restrict__ ows,
    const uint16_t* __restrict__ woA, const uint16_t* __restrict__ woB, unsigned* __restrict__ cnt,
    const float* __restrict__ slab, const uint16_t* __restrict__ outp) {
  extern __shared__ __attribute__((aligned(16))) char smem[];
  const int tid = threadIdx.x, lane = tid & 63;
  const int w = __builtin_amdgcn_readfirstlane(tid >> 6);
  const int bid = blockIdx.x, xcd = bid & 7, idx = bid >> 3;
  const int X = xcd >> 2, h = (xcd & 3) * 8 + (idx & 7), bg = idx >> 3;
  const int c = w & 3;
  if (tid == 0) cnt[bid * 16] = 0u;
  const int r = lane & 31, hh = lane >> 5, blk = (lane >> 4) & 1, q = (lane & 15) >> 2, p = lane & 3;
  const int troff = (8 * hh + q) * 256 + 32 * blk + 16 * (p & 1) + 8 * (p >> 1);
  int tro[4];
#pragma unroll
  for (int t = 0; t < 4; ++t) tro[t] = troff + ((t ^ q) << 6);
  bf16x8 qf[8];
  unsigned pfk0 = 0, pfk1 = 0;

  if (w < 4) {
    f32x16 acc[4][2];
#pragma unroll
    for (int t = 0; t < 4; ++t)
#pragma unroll
      for (int u = 0; u < 2; ++u)
#pragma unroll
        for (int i = 0; i < 16; ++i) acc[t][u][i] = 0.f;
    const int f = (r >> 1) & 7;
    int xo[4];
#pragma unroll
    for (int s = 0; s < 4; ++s) xo[s] = 16384 + c * 8192 + r * 128 + (((2 * s + hh) ^ f) << 4);
    bf16x8 af[2][4], bf[2][2];
    auto rdA = [&](const char* lb, int s, int slot, int t) {
      af[slot][t] = tr_frag(lb + tro[t] + s * 4096, lb + tro[t] + s * 4096 + 1024);
    };
    auto rdB = [&](const char* lb, int s, int slot) {
      bf[slot][0] = *(const bf16x8*)(lb + xo[s]);
      bf[slot][1] = *(const bf16x8*)(lb + xo[s] + 4096);
    };
    auto kstep = [&](int slot, const char* lb, int s) {
      const int ns = slot ^ 1;
      acc[0][0] = MFMA32(af[slot][0], bf[slot][0], acc[0][0]); SCHED0();
      rdB(lb, s, ns); SCHED0();
      acc[0][1] = MFMA32(af[slot][0], bf[slot][1], acc[0][1]); SCHED0();
      rdA(lb, s, ns, 0); SCHED0();
      acc[1][0] = MFMA32(af[slot][1], bf[slot][0], acc[1][0]); SCHED0();
      rdA(lb, s, ns, 1); SCHED0();
      acc[1][1] = MFMA32(af[slot][1], bf[slot][1], acc[1][1]); SCHED0();
      rdA(lb, s, ns, 2); SCHED0();
      acc[2][0] = MFMA32(af[slot][2], bf[slot][0], acc[2][0]); SCHED0();
      rdA(lb, s, ns, 3); SCHED0();
      acc[2][1] = MFMA32(af[slot][2], bf[slot][1], acc[2][1]);
      acc[3][0] = MFMA32(af[slot][3], bf[slot][0], acc[3][0]);
      acc[3][1] = MFMA32(af[slot][3], bf[slot][1], acc[3][1]); SCHED0();
    };
    unsigned tch, tch2;
    {
      const int u = bg * 8 + c * 2 + (lane & 1), sel = (lane >> 1) & 3;
      const char* pk = (const char*)((X ? kB : kA) + (size_t)(u * 32 + h) * 4096);
      const char* pv = (const char*)((X ? vB : vA) + (size_t)(u * 32 + h) * 4096);
      const char* po = (const char*)(ows + (size_t)((X * 32 + u) * 256 + h * 8) * 512);
      const char* pt = sel == 0 ? pk : (sel == 1 ? pv : po);
      if (sel == 3) {
        const int hg = xcd & 3, tile = (X * 8 + (idx >> 2)) * 4 + (idx & 3);
        const int sl = (lane & 1) ? (tile * 4 + hg) * 3 + (c < 3 ? c : 2)
                                  : (tile * 4 + c) * 3 + (hg < c ? hg : (hg > 0 ? hg - 1 : 0));
        pt = (const char*)(slab + (size_t)sl * SLAB_STRIDE + 4096);
        if ((lane & 1) && c == 3) pt = (const char*)(outp + (size_t)(((idx >> 2) * 4 + hg) * 32 + 16) * 1024);
      }
      asm volatile("global_load_dword %0, %1, off nt" : "=v"(tch) : "v"(pt) : "memory");
      const char* p2 = (c < 2) ? (const char*)((X ? wqB : wqA) + (size_t)(lane * 8 + c * 4) * 4096 + h * 128)
                               : (const char*)((X ? xB : xA) + (size_t)(bg * 256) * 512) + lane * 4096 + (c - 2) * 512;
      asm volatile("global_load_dword %0, %1, off nt" : "=v"(tch2) : "v"(p2) : "memory");
    }
    __builtin_amdgcn_s_barrier();
    MEMFENCE();
    rdB(smem, 0, 0);
#pragma unroll
    for (int t = 0; t < 4; ++t) rdA(smem, 0, 0, t);
    SCHED0();
    unsigned cur = 0;
#pragma unroll 1
    for (int st = 0; st < 8; ++st) {
      const unsigned nxt = (cur == 2 * K1_STAGE) ? 0u : cur + K1_STAGE;
      const char* lb = smem + cur;
      kstep(0, lb, 1);
      kstep(1, lb, 2);
      kstep(0, lb, 3);
      asm volatile("s_waitcnt lgkmcnt(0)" ::: "memory");
      __builtin_amdgcn_s_barrier();
      MEMFENCE();
      kstep(1, smem + nxt, 0);
      cur = nxt;
    }
    asm volatile("s_waitcnt vmcnt(0)" : "+v"(tch), "+v"(tch2)::"memory");
    u32x4* qd = (u32x4*)(smem + 65536 + c * 8192) + lane;
#pragma unroll
    for (int t = 0; t < 4; ++t)
#pragma unroll
      for (int s = 0; s < 2; ++s) {
        qd[(2 * t + s) * 64] = __builtin_bit_cast(u32x4, pack8(acc[t][1], s));
        qf[2 * t + s] = pack8(acc[t][0], s);
      }
    asm volatile("s_waitcnt lgkmcnt(0)" ::: "memory");
    __builtin_amdgcn_s_barrier();
    MEMFENCE();
  } else {
    const uint16_t* x = X ? xB : xA;
    const uint16_t* kc = X ? kB : kA;
    const uint16_t* vc = X ? vB : vA;
    const uint16_t* wq = X ? wqB : wqA;
    const char* xb = (const char*)(x + (size_t)((bg * 8 + c * 2) * 32) * 512);
    const char* wb = (const char*)(wq + (size_t)(c * 16) * 4096 + h * 128);
    const unsigned xoff0 = (lane >> 3) * 1024 + (((lane & 7) ^ (lane >> 4)) << 4);
    const unsigned xoff1 = (8 + (lane >> 3)) * 1024 + (((lane & 7) ^ ((lane >> 4) | 4)) << 4);
    const unsigned woff = (lane >> 4) * 8192 + (((lane & 15) ^ ((lane >> 4) << 2)) << 4);
    const unsigned sbase = lds_addr(smem);
    auto stage = [&](int st, unsigned bufoff) {
      const unsigned lb = sbase + bufoff;
      const char* wg = wb + (size_t)st * (64 * 8192);
#pragma unroll
      for (int j = 0; j < 4; ++j) glds16<1>(wg + j * (4 * 8192), woff, lb + c * 4096 + j * 1024);
      const unsigned lx = lb + 16384 + c * 8192;
      const char* xg = xb + st * 128;
#pragma unroll
      for (int j = 0; j < 4; ++j) {
        glds16<0>(xg + j * 16384, xoff0, lx + j * 2048);
        glds16<0>(xg + j * 16384, xoff1, lx + j * 2048 + 1024);
      }
    };
    const unsigned kvo = lane * 16;
    u32x4 kv[2][16];
#define KVNT " nt"
    auto kvchunk = [&](int ch) {
      const int isv = ch >> 2, u = (ch >> 1) & 1, o = isv * 8 + (ch & 1) * 4;
      const uint16_t* g = (isv ? vc : kc) + (size_t)((bg * 8 + c * 2 + u) * 32 + h) * 4096 + (ch & 1) * 2048;
      asm volatile(
          "global_load_dwordx4 %0, %4, %5" KVNT "\n\t"
          "global_load_dwordx4 %1, %4, %5 offset:1024" KVNT "\n\t"
          "global_load_dwordx4 %2, %4, %5 offset:2048" KVNT "\n\t"
          "global_load_dwordx4 %3, %4, %5 offset:3072" KVNT
          : "=v"(kv[u][o]), "=v"(kv[u][o + 1]), "=v"(kv[u][o + 2]), "=v"(kv[u][o + 3])
          : "v"(kvo), "s"(g) : "memory");
    };
#define KV_PIN(u, o, N) asm volatile("s_waitcnt vmcnt(%8)" : "+v"(kv[u][o]), "+v"(kv[u][o + 1]), "+v"(kv[u][o + 2]), "+v"(kv[u][o + 3]), \
                                     "+v"(kv[u][o + 4]), "+v"(kv[u][o + 5]), "+v"(kv[u][o + 6]), "+v"(kv[u][o + 7]) : "i"(N) : "memory")
    auto putK = [&](int u) {
      char* kl = smem + 98304 + (c * 2 + u) * 8192 + (lane >> 4) * 256;
#pragma unroll
      for (int i = 0; i < 8; ++i) *(u32x4*)(kl + i * 1024 + (((lane & 15) ^ ((4 * i + (lane >> 4)) & 15)) << 4)) = kv[u][i];
    };
    auto putV = [&](int u) {
      char* vl = smem + u * 32768 + c * 8192 + (lane >> 4) * 256 + (((lane & 15) ^ ((lane >> 4) << 2)) << 4);
#pragma unroll
      for (int i = 0; i < 8; ++i) *(u32x4*)(vl + i * 1024) = kv[u][8 + i];
    };
    stage(0, 0);
    stage(1, K1_STAGE);
    stage(2, 2 * K1_STAGE);
    kvchunk(0);
    kvchunk(1);
    asm volatile("s_waitcnt vmcnt(32)" ::: "memory");
    __builtin_amdgcn_s_barrier();
    MEMFENCE();
    unsigned pf0 = 0, pf1 = 0;
#pragma unroll
    for (int st = 0; st < 8; ++st) {
      if (st == 1) asm volatile("s_waitcnt vmcnt(24)" ::: "memory");
      else if (st < 6) asm volatile("s_waitcnt vmcnt(20)" ::: "memory");
      else if (st == 6) asm volatile("s_waitcnt vmcnt(8)" ::: "memory");
      __builtin_amdgcn_s_barrier();
      MEMFENCE();
      if (st < 5) stage(st + 3, (st % 3) * K1_STAGE);
      if (st < 6) kvchunk(st + 2);
      if (st == 5) {
        KV_PIN(0, 0, 24); KV_PIN(1, 0, 24);
        putK(0); putK(1);
      }
      if (st == 6) {
        const char* pw = (const char*)(X ? woB : woA) + (size_t)((xcd & 3) * 32 + idx) * 32768 + c * 8192;
        asm volatile("global_load_dword %0, %2, %3\n\tglobal_load_dword %1, %2, %3 offset:64"
                     : "=v"(pf0), "=v"(pf1) : "v"(lane * 128), "s"(pw) : "memory");
        KV_PIN(0, 8, 10);
        putV(0);
      }
    }
    KV_PIN(1, 8, 2);
    putV(1);
    pfk0 = pf0; pfk1 = pf1;
    asm volatile("s_waitcnt lgkmcnt(0)" ::: "memory");
    __builtin_amdgcn_s_barrier();
    MEMFENCE();
    const u32x4* qs = (const u32x4*)(smem + 65536 + c * 8192) + lane;
#pragma unroll
    for (int i = 0; i < 8; ++i) qf[i] = __builtin_bit_cast(bf16x8, qs[i * 64]);
  }

  const int unit = c * 2 + (w >> 2), b = bg * 8 + unit;
  const char* kl = smem + 98304 + unit * 8192;
  const char* vl = smem + (w >> 2) * 32768 + c * 8192;
  const int pr = (r & 0x13) | ((r & 4) << 1) | ((r & 8) >> 1);
  f32x16 sacc, sac1;
#pragma unroll
  for (int i = 0; i < 16; ++i) { sacc[i] = 0.f; sac1[i] = 0.f; }
#pragma unroll
  for (int i = 0; i < 8; i += 2) {
    sacc = MFMA32(*(const bf16x8*)(kl + pr * 256 + (((2 * i + hh) ^ (pr & 15)) << 4)), qf[i], sacc);
    sac1 = MFMA32(*(const bf16x8*)(kl + pr * 256 + (((2 * i + 2 + hh) ^ (pr & 15)) << 4)), qf[i + 1], sac1);
  }
  sacc += sac1;

  const float scale = 0.08838834764831845f;
  float m = -3.0e38f;
#pragma unroll
  for (int i = 0; i < 16; i += 2) {
    unsigned u = cvtpk(sacc[i], sacc[i + 1]);
    float a = __uint_as_float(u << 16) * scale, cc = __uint_as_float(u & 0xffff0000u) * scale;
    unsigned u2 = cvtpk(a, cc);
    sacc[i] = __uint_as_float(u2 << 16);
    sacc[i + 1] = __uint_as_float(u2 & 0xffff0000u);
    m = fmaxf(m, fmaxf(sacc[i], sacc[i + 1]));
  }
  {
    auto rr = __builtin_amdgcn_permlane32_swap(__float_as_uint(m), __float_as_uint(m), false, false);
    m = fmaxf(__uint_as_float(rr[0]), __uint_as_float(rr[1]));
  }
  float sum = 0.f;
#pragma unroll
  for (int i = 0; i < 16; ++i) {
    sacc[i] = __builtin_amdgcn_exp2f((sacc[i] - m) * 1.4426950408889634f);
    sum += sacc[i];
  }
  {
    auto rr = __builtin_amdgcn_permlane32_swap(__float_as_uint(sum), __float_as_uint(sum), false, false);
    sum = __uint_as_float(rr[0]) + __uint_as_float(rr[1]);
  }
  const float inv = 1.0f / sum;
#pragma unroll
  for (int i = 0; i < 16; ++i) sacc[i] *= inv;
  bf16x8 pf0 = pack8(sacc, 0), pf1 = pack8(sacc, 1);

  u32x4* od = (u32x4*)(ows + ((size_t)((X * 32 + b) * 256 + h * 8) * 64 + lane) * 8);
#pragma unroll
  for (int t = 0; t < 4; ++t) {
    f32x16 o;
#pragma unroll
    for (int i = 0; i < 16; ++i) o[i] = 0.f;
    const char* pa = vl + tro[t];
    o = MFMA32(tr_frag(pa, pa + 1024), pf0, o);
    o = MFMA32(tr_frag(pa + 4096, pa + 4096 + 1024), pf1, o);
    {
      u32x4 v0 = __builtin_bit_cast(u32x4, pack8(o, 0)), v1 = __builtin_bit_cast(u32x4, pack8(o, 1));
      asm volatile("global_store_dwordx4 %0, %1, off sc1\n\tglobal_store_dwordx4 %0, %2, off offset:1024 sc1\n\ts_nop 1"
                   ::"v"(od + (2 * t) * 64), "v"(v0), "v"(v1) : "memory");
    }
  }
  asm volatile("s_waitcnt vmcnt(0)" : "+v"(pfk0), "+v"(pfk1)::"memory");
}

constexpr int K2_NB = 8;
constexpr int K2_LDS = K2_NB * 16384;
constexpr int K2_OD = 16;

__global__ __launch_bounds__(512, 2) void k2_oproj(const uint16_t* __restrict__ woA, const uint16_t* __restrict__ woB,
                                                   const uint16_t* __restrict__ ows, float* __restrict__ slab,
                                                   unsigned* __restrict__ cnt, uint16_t* __restrict__ out) {
  extern __shared__ __attribute__((aligned(16))) char smem[];
  const int tid = threadIdx.x, lane = tid & 63;
  const int w = __builtin_amdgcn_readfirstlane(tid >> 6);
  const int bid = blockIdx.x, xcd = bid & 7, idx = bid >> 3;
  const int X = xcd >> 2, hg = xcd & 3, rt = idx >> 2, ct = idx & 3;
  const int c = w & 3;
  const int tile = (X * 8 + rt) * 4 + ct;

  if (w < 4) {
    const int hh = lane >> 5, blk = (lane >> 4) & 1, q = (lane & 15) >> 2, p = lane & 3;
    const int troff = (8 * hh + q) * 256 + 32 * blk + 16 * (p & 1) + 8 * (p >> 1);
    unsigned pa[2][4];
#pragma unroll
    for (int t = 0; t < 4; ++t) {
      pa[0][t] = lds_addr(smem) + troff + ((t ^ q) << 6);
      pa[1][t] = pa[0][t] + 65536;
      asm volatile("" : "+v"(pa[0][t]), "+v"(pa[1][t]));
    }
    const char* ob = (const char*)ows + (size_t)((X * 32 + rt * 4 + c) * 256 + hg * 64) * 1024;
    const unsigned oo = lane * 16;
    u32x4 of[K2_OD];
    f32x16 acc[4];
#pragma unroll
    for (int t = 0; t < 4; ++t)
#pragma unroll
      for (int i = 0; i < 16; ++i) acc[t][i] = 0.f;
    bf16x8 af[2][4];
    auto oload = [&](int ks) {
      asm volatile("global_load_dwordx4 %0, %1, %2" : "=v"(of[ks % K2_OD]) : "v"(oo), "s"(ob + ks * 1024) : "memory");
    };
    auto rdA = [&](int ks, int t) {
      const int o = ((ks >> 2) % K2_NB) * 16384 + (ks & 3) * 4096;
      const unsigned a = pa[o >> 16][t] + (o & 65535);
      s16x4 lo = __builtin_amdgcn_ds_read_tr16_b64_v4i16((lds_s16x4_ptr)(size_t)a);
      s16x4 hi = __builtin_amdgcn_ds_read_tr16_b64_v4i16((lds_s16x4_ptr)(size_t)(a + 1024));
      af[ks & 1][t] = __builtin_bit_cast(bf16x8, __builtin_shufflevector(lo, hi, 0, 1, 2, 3, 4, 5, 6, 7));
    };
#pragma unroll
    for (int ks = 0; ks < K2_OD; ++ks) oload(ks);
    __builtin_amdgcn_s_barrier();
    MEMFENCE();
#pragma unroll
    for (int t = 0; t < 4; ++t) rdA(0, t);
    SCHED0();
#pragma unroll
    for (int ks = 0; ks < 64; ++ks) {
      if ((ks & 3) == 3 && ks < 63) {
        asm volatile("s_waitcnt lgkmcnt(0)" ::: "memory");
        __builtin_amdgcn_s_barrier();
        MEMFENCE();
      }
      {
        constexpr int dummy = 0; (void)dummy;
        const int n = (63 - ks < K2_OD - 1) ? 63 - ks : K2_OD - 1;
        asm volatile("s_waitcnt vmcnt(%1)" : "+v"(of[ks % K2_OD]) : "i"(n) : "memory");
      }
      const bf16x8 bfr = __builtin_bit_cast(bf16x8, of[ks % K2_OD]);
      acc[0] = MFMA32(af[ks & 1][0], bfr, acc[0]); SCHED0();
      if (ks < 63) { rdA(ks + 1, 0); rdA(ks + 1, 1); } SCHED0();
      acc[1] = MFMA32(af[ks & 1][1], bfr, acc[1]); SCHED0();
      if (ks < 63) { rdA(ks + 1, 2); rdA(ks + 1, 3); } SCHED0();
      acc[2] = MFMA32(af[ks & 1][2], bfr, acc[2]);
      acc[3] = MFMA32(af[ks & 1][3], bfr, acc[3]); SCHED0();
      if (ks + K2_OD < 64) oload(ks + K2_OD);
      SCHED0();
    }
    if (c == hg) {
      f32x4* sd = (f32x4*)smem + lane;
#pragma unroll
      for (int t = 0; t < 4; ++t)
#pragma unroll
        for (int g = 0; g < 4; ++g) sd[(t * 4 + g) * 64] = f32x4{acc[t][4 * g], acc[t][4 * g + 1], acc[t][4 * g + 2], acc[t][4 * g + 3]};
      asm volatile("s_waitcnt lgkmcnt(0)" ::: "memory");
    } else {
      const float* dst = slab + ((size_t)(tile * 4 + c) * 3 + (hg < c ? hg : hg - 1)) * SLAB_STRIDE;
      const unsigned so = lane * 16;
      asm volatile("s_nop 7\n\ts_nop 7" ::: "memory");
#pragma unroll
      for (int t = 0; t < 4; ++t) {
        f32x4 v0 = {acc[t][0], acc[t][1], acc[t][2], acc[t][3]}, v1 = {acc[t][4], acc[t][5], acc[t][6], acc[t][7]};
        f32x4 v2 = {acc[t][8], acc[t][9], acc[t][10], acc[t][11]}, v3 = {acc[t][12], acc[t][13], acc[t][14], acc[t][15]};
        asm volatile("global_store_dwordx4 %0, %2, %1 sc1\n\t"
                     "global_store_dwordx4 %0, %3, %1 offset:1024 sc1\n\t"
                     "global_store_dwordx4 %0, %4, %1 offset:2048 sc1\n\t"
                     "global_store_dwordx4 %0, %5, %1 offset:3072 sc1\n\ts_nop 1"
                     ::"v"(so), "s"(dst + t * 1024), "v"(v0), "v"(v1), "v"(v2), "v"(v3) : "memory");
      }
      asm volatile("s_waitcnt vmcnt(0)" ::: "memory");
      if (lane == 0) __hip_atomic_fetch_add(cnt + (tile * 4 + c) * 16, 1u, __ATOMIC_RELAXED, __HIP_MEMORY_SCOPE_AGENT);
    }
  } else {
    const uint16_t* wo = X ? woB : woA;
    const char* wb = (const char*)(wo + (size_t)(hg * 1024 + c * 16) * 512 + ct * 128);
    const unsigned woff = (lane >> 4) * 1024 + (((lane & 15) ^ ((lane >> 4) << 2)) << 4);
    const unsigned sbase = lds_addr(smem) + c * 4096;
    auto stage = [&](int st) {
#pragma unroll
      for (int j = 0; j < 4; ++j) glds16<0>(wb + (size_t)st * 65536 + j * 4096, woff, sbase + (st % K2_NB) * 16384 + j * 1024);
    };
#pragma unroll
    for (int st = 0; st < K2_NB; ++st) stage(st);
    asm volatile("s_waitcnt vmcnt(%0)" ::"i"(4 * (K2_NB - 1)) : "memory");
    __builtin_amdgcn_s_barrier();
    MEMFENCE();
#pragma unroll
    for (int st = 0; st < 15; ++st) {
      constexpr int dummy = 0; (void)dummy;
      const int last = (st + K2_NB - 1 < 15) ? st + K2_NB - 1 : 15;
      asm volatile("s_waitcnt vmcnt(%0)" ::"i"(4 * (last - (st + 1))) : "memory");
      __builtin_amdgcn_s_barrier();
      MEMFENCE();
      if (st + K2_NB < 16) stage(st + K2_NB);
    }
    if (w == 4) {
      const unsigned* cp = cnt + (tile * 4 + hg) * 16;
      for (int spin = 0; spin < (1 << 20); ++spin) {
        if (__hip_atomic_load(cp, __ATOMIC_RELAXED, __HIP_MEMORY_SCOPE_AGENT) >= 3u) break;
        __builtin_amdgcn_s_sleep(2);
      }
    }
  }
  MEMFENCE();
  __builtin_amdgcn_s_barrier();
  MEMFENCE();
  {
    const float* src = slab + (size_t)(tile * 4 + hg) * 3 * SLAB_STRIDE + w * 512;
    const unsigned so = lane * 16;
    f32x4 a0, a1, b0, b1, c0, c1;
    asm volatile("global_load_dwordx4 %0, %6, %7 sc1\n\t"
                 "global_load_dwordx4 %1, %6, %7 offset:1024 sc1\n\t"
                 "global_load_dwordx4 %2, %6, %8 sc1\n\t"
                 "global_load_dwordx4 %3, %6, %8 offset:1024 sc1\n\t"
                 "global_load_dwordx4 %4, %6, %9 sc1\n\t"
                 "global_load_dwordx4 %5, %6, %9 offset:1024 sc1\n\t"
                 "s_waitcnt vmcnt(0)"
                 : "=&v"(a0), "=&v"(a1), "=&v"(b0), "=&v"(b1), "=&v"(c0), "=&v"(c1)
                 : "v"(so), "s"(src), "s"(src + SLAB_STRIDE), "s"(src + 2 * SLAB_STRIDE) : "memory");
    const f32x4* own = (const f32x4*)smem + (w * 2) * 64 + lane;
    const f32x4 o0 = own[0], o1 = own[64];
    f32x4 s0, s1;
    if (hg == 0) { s0 = ((o0 + a0) + b0) + c0; s1 = ((o1 + a1) + b1) + c1; }
    else if (hg == 1) { s0 = ((a0 + o0) + b0) + c0; s1 = ((a1 + o1) + b1) + c1; }
    else if (hg == 2) { s0 = ((a0 + b0) + o0) + c0; s1 = ((a1 + b1) + o1) + c1; }
    else { s0 = ((a0 + b0) + c0) + o0; s1 = ((a1 + b1) + c1) + o1; }
    u32x4 o = {cvtpk(s0[0], s0[1]), cvtpk(s0[2], s0[3]), cvtpk(s1[0], s1[1]), cvtpk(s1[2], s1[3])};
    uint16_t* dst = out + (size_t)((rt * 4 + hg) * 32 + (lane & 31)) * 1024 + X * 512 + ct * 128 + (w >> 1) * 32 + (w & 1) * 16 + (lane >> 5) * 8;
    *(u32x4*)dst = o;
  }
}

extern "C" void kernel_launch(void* const* d_in, const int* in_sizes, int n_in, void* d_out, int out_size,
                              void* d_ws, size_t ws_size, hipStream_t stream) {
  (void)in_sizes; (void)n_in; (void)out_size; (void)ws_size;
  (void)hipFuncSetAttribute((const void*)k1_qproj_attn, hipFuncAttributeMaxDynamicSharedMemorySize, K1_LDS);
  (void)hipFuncSetAttribute((const void*)k2_oproj, hipFuncAttributeMaxDynamicSharedMemorySize, K2_LDS);
  const uint16_t* xA = (const uint16_t*)d_in[0];
  const uint16_t* kA = (const uint16_t*)d_in[1];
  const uint16_t* vA = (const uint16_t*)d_in[2];
  const uint16_t* wqA = (const uint16_t*)d_in[3];
  const uint16_t* woA = (const uint16_t*)d_in[4];
  const uint16_t* xB = (const uint16_t*)d_in[5];
  const uint16_t* kB = (const uint16_t*)d_in[6];
  const uint16_t* vB = (const uint16_t*)d_in[7];
  const uint16_t* wqB = (const uint16_t*)d_in[8];
  const uint16_t* woB = (const uint16_t*)d_in[9];
  uint16_t* ows = (uint16_t*)d_ws;
  float* slab = (float*)((char*)d_ws + (16u << 20));
  unsigned* cnt = (unsigned*)((char*)d_ws + (32u << 20));
  k1_qproj_attn<<<256, 512, K1_LDS, stream>>>(xA, kA, vA, wqA, xB, kB, vB, wqB, ows, woA, woB, cnt, slab, (const uint16_t*)d_out);
  k2_oproj<<<256, 512, K2_LDS, stream>>>(woA, woB, ows, slab, cnt, (uint16_t*)d_out);
}
```

```cpp
#include <hip/hip_runtime.h>
#include <hip/hip_bf16.h>
#include <stdint.h>

typedef __attribute__((ext_vector_type(8))) __bf16 bf16x8;
typedef __attribute__((ext_vector_type(2))) __bf16 bf16x2;
typedef __attribute__((ext_vector_type(4))) short s16x4;
typedef __attribute__((ext_vector_type(2))) float f32x2;
typedef __attribute__((ext_vector_type(4))) float f32x4;
typedef __attribute__((ext_vector_type(16))) float f32x16;
typedef __attribute__((ext_vector_type(4))) unsigned u32x4;
typedef __attribute__((address_space(3))) s16x4* lds_s16x4_ptr;

#define DI __device__ __forceinline__
#define MFMA32(a, b, c) __builtin_amdgcn_mfma_f32_32x32x16_bf16((a), (b), (c), 0, 0, 0)
#define MEMFENCE() asm volatile("" ::: "memory")
#define SCHED0() __builtin_amdgcn_sched_barrier(0)

DI unsigned cvtpk(float lo, float hi) {
  f32x2 v = {lo, hi};
  bf16x2 r = __builtin_convertvector(v, bf16x2);
  return __builtin_bit_cast(unsigned, r);
}
DI bf16x8 pack8(const f32x16& x, int s) {
  u32x4 p = {cvtpk(x[8 * s + 0], x[8 * s + 1]), cvtpk(x[8 * s + 2], x[8 * s + 3]),
             cvtpk(x[8 * s + 4], x[8 * s + 5]), cvtpk(x[8 * s + 6], x[8 * s + 7])};
  return __builtin_bit_cast(bf16x8, p);
}
DI s16x4 tr_read(const char* p) { return __builtin_amdgcn_ds_read_tr16_b64_v4i16((lds_s16x4_ptr)(p)); }
DI bf16x8 tr_frag(const char* p0, const char* p1) {
  s16x4 lo = tr_read(p0), hi = tr_read(p1);
  return __builtin_bit_cast(bf16x8, __builtin_shufflevector(lo, hi, 0, 1, 2, 3, 4, 5, 6, 7));
}
template <int NT>
DI void glds16(const void* gbase, unsigned voff, unsigned ldst) {
  unsigned keep;
  if constexpr (NT)
    asm volatile("s_mov_b32 %0, m0\n\ts_mov_b32 m0, %2\n\ts_nop 0\n\tglobal_load_lds_dwordx4 %1, %3 nt\n\ts_mov_b32 m0, %0"
                 : "=&s"(keep) : "v"(voff), "s"(ldst), "s"(gbase) : "memory");
  else
    asm volatile("s_mov_b32 %0, m0\n\ts_mov_b32 m0, %2\n\ts_nop 0\n\tglobal_load_lds_dwordx4 %1, %3\n\ts_mov_b32 m0, %0"
                 : "=&s"(keep) : "v"(voff), "s"(ldst), "s"(gbase) : "memory");
}
DI unsigned lds_addr(const void* p) { return (unsigned)(size_t)(__attribute__((address_space(3))) const char*)p; }

constexpr int SLAB_STRIDE = 4096 + 32;
constexpr int K1_STAGE = 49152;
constexpr int K1_LDS = 163840;

__global__ __launch_bounds__(512, 2) void k1_qproj_attn(
    const uint16_t* __restrict__ xA, const uint16_t* __restrict__ kA, const uint16_t* __restrict__ vA,
    const uint16_t* __restrict__ wqA, const uint16_t* __restrict__ xB, const uint16_t* __restrict__ kB,
    const uint16_t* __restrict__ vB, const uint16_t* __restrict__ wqB, uint16_t* __restrict__ ows,
    const uint16_t* __restrict__ woA, const uint16_t* __restrict__ woB, unsigned* __restrict__ cnt,
    const float* __restrict__ slab, const uint16_t* __restrict__ outp) {
  extern __shared__ __attribute__((aligned(16))) char smem[];
  const int tid = threadIdx.x, lane = tid & 63;
  const int w = __builtin_amdgcn_readfirstlane(tid >> 6);
  const int bid = blockIdx.x, xcd = bid & 7, idx = bid >> 3;
  const int X = xcd >> 2, h = (xcd & 3) * 8 + (idx & 7), bg = idx >> 3;
  const int c = w & 3;
  if (tid == 0) cnt[bid * 16] = 0u;
  const int r = lane & 31, hh = lane >> 5, blk = (lane >> 4) & 1, q = (lane & 15) >> 2, p = lane & 3;
  const int troff = (8 * hh + q) * 256 + 32 * blk + 16 * (p & 1) + 8 * (p >> 1);
  int tro[4];
#pragma unroll
  for (int t = 0; t < 4; ++t) tro[t] = troff + ((t ^ q) << 6);
  bf16x8 qf[8];
  unsigned pfk0 = 0, pfk1 = 0;

  if (w < 4) {
    f32x16 acc[4][2];
#pragma unroll
    for (int t = 0; t < 4; ++t)
#pragma unroll
      for (int u = 0; u < 2; ++u)
#pragma unroll
        for (int i = 0; i < 16; ++i) acc[t][u][i] = 0.f;
    const int f = (r >> 1) & 7;
    int xo[4];
#pragma unroll
    for (int s = 0; s < 4; ++s) xo[s] = 16384 + c * 8192 + r * 128 + (((2 * s + hh) ^ f) << 4);
    bf16x8 af[2][4], bf[2][2];
    auto rdA = [&](const char* lb, int s, int slot, int t) {
      af[slot][t] = tr_frag(lb + tro[t] + s * 4096, lb + tro[t] + s * 4096 + 1024);
    };
    auto rdB = [&](const char* lb, int s, int slot) {
      bf[slot][0] = *(const bf16x8*)(lb + xo[s]);
      bf[slot][1] = *(const bf16x8*)(lb + xo[s] + 4096);
    };
    auto kstep = [&](int slot, const char* lb, int s) {
      const int ns = slot ^ 1;
      acc[0][0] = MFMA32(af[slot][0], bf[slot][0], acc[0][0]); SCHED0();
      rdB(lb, s, ns); SCHED0();
      acc[0][1] = MFMA32(af[slot][0], bf[slot][1], acc[0][1]); SCHED0();
      rdA(lb, s, ns, 0); SCHED0();
      acc[1][0] = MFMA32(af[slot][1], bf[slot][0], acc[1][0]); SCHED0();
      rdA(lb, s, ns, 1); SCHED0();
      acc[1][1] = MFMA32(af[slot][1], bf[slot][1], acc[1][1]); SCHED0();
      rdA(lb, s, ns, 2); SCHED0();
      acc[2][0] = MFMA32(af[slot][2], bf[slot][0], acc[2][0]); SCHED0();
      rdA(lb, s, ns, 3); SCHED0();
      acc[2][1] = MFMA32(af[slot][2], bf[slot][1], acc[2][1]);
      acc[3][0] = MFMA32(af[slot][3], bf[slot][0], acc[3][0]);
      acc[3][1] = MFMA32(af[slot][3], bf[slot][1], acc[3][1]); SCHED0();
    };
    unsigned tch;
    {
      const int u = bg * 8 + c * 2 + (lane & 1), sel = (lane >> 1) & 3;
      const char* pk = (const char*)((X ? kB : kA) + (size_t)(u * 32 + h) * 4096);
      const char* pv = (const char*)((X ? vB : vA) + (size_t)(u * 32 + h) * 4096);
      const char* po = (const char*)(ows + (size_t)((X * 32 + u) * 256 + h * 8) * 512);
      const char* pt = sel == 0 ? pk : (sel == 1 ? pv : po);
      if (sel == 3) {
        const int hg = xcd & 3, tile = (X * 8 + (idx >> 2)) * 4 + (idx & 3);
        const int sl = (lane & 1) ? (tile * 4 + hg) * 3 + (c < 3 ? c : 2)
                                  : (tile * 4 + c) * 3 + (hg < c ? hg : (hg > 0 ? hg - 1 : 0));
        pt = (const char*)(slab + (size_t)sl * SLAB_STRIDE + 4096);
        if ((lane & 1) && c == 3) pt = (const char*)(outp + (size_t)(((idx >> 2) * 4 + hg) * 32 + 16) * 1024);
      }
      asm volatile("global_load_dword %0, %1, off nt" : "=v"(tch) : "v"(pt) : "memory");
    }
    __builtin_amdgcn_s_barrier();
    MEMFENCE();
    rdB(smem, 0, 0);
#pragma unroll
    for (int t = 0; t < 4; ++t) rdA(smem, 0, 0, t);
    SCHED0();
    unsigned cur = 0;
#pragma unroll 1
    for (int st = 0; st < 8; ++st) {
      const unsigned nxt = (cur == 2 * K1_STAGE) ? 0u : cur + K1_STAGE;
      const char* lb = smem + cur;
      kstep(0, lb, 1);
      kstep(1, lb, 2);
      kstep(0, lb, 3);
      asm volatile("s_waitcnt lgkmcnt(0)" ::: "memory");
      __builtin_amdgcn_s_barrier();
      MEMFENCE();
      kstep(1, smem + nxt, 0);
      cur = nxt;
    }
    asm volatile("s_waitcnt vmcnt(0)" : "+v"(tch)::"memory");
    u32x4* qd = (u32x4*)(smem + 65536 + c * 8192) + lane;
#pragma unroll
    for (int t = 0; t < 4; ++t)
#pragma unroll
      for (int s = 0; s < 2; ++s) {
        qd[(2 * t + s) * 64] = __builtin_bit_cast(u32x4, pack8(acc[t][1], s));
        qf[2 * t + s] = pack8(acc[t][0], s);
      }
    asm volatile("s_waitcnt lgkmcnt(0)" ::: "memory");
    __builtin_amdgcn_s_barrier();
    MEMFENCE();
  } else {
    const uint16_t* x = X ? xB : xA;
    const uint16_t* kc = X ? kB : kA;
    const uint16_t* vc = X ? vB : vA;
    const uint16_t* wq = X ? wqB : wqA;
    const char* xb = (const char*)(x + (size_t)((bg * 8 + c * 2) * 32) * 512);
    const char* wb = (const char*)(wq + (size_t)(c * 16) * 4096 + h * 128);
    const unsigned xoff0 = (lane >> 3) * 1024 + (((lane & 7) ^ (lane >> 4)) << 4);
    const unsigned xoff1 = (8 + (lane >> 3)) * 1024 + (((lane & 7) ^ ((lane >> 4) | 4)) << 4);
    const unsigned woff = (lane >> 4) * 8192 + (((lane & 15) ^ ((lane >> 4) << 2)) << 4);
    const unsigned sbase = lds_addr(smem);
    auto stage = [&](int st, unsigned bufoff) {
      const unsigned lb = sbase + bufoff;
      const char* wg = wb + (size_t)st * (64 * 8192);
#pragma unroll
      for (int j = 0; j < 4; ++j) glds16<1>(wg + j * (4 * 8192), woff, lb + c * 4096 + j * 1024);
      const unsigned lx = lb + 16384 + c * 8192;
      const char* xg = xb + st * 128;
#pragma unroll
      for (int j = 0; j < 4; ++j) {
        glds16<0>(xg + j * 16384, xoff0, lx + j * 2048);
        glds16<0>(xg + j * 16384, xoff1, lx + j * 2048 + 1024);
      }
    };
    const unsigned kvo = lane * 16;
    u32x4 kv[2][16];
#define KVNT " nt"
    auto kvchunk = [&](int ch) {
      const int isv = ch >> 2, u = (ch >> 1) & 1, o = isv * 8 + (ch & 1) * 4;
      const uint16_t* g = (isv ? vc : kc) + (size_t)((bg * 8 + c * 2 + u) * 32 + h) * 4096 + (ch & 1) * 2048;
      asm volatile(
          "global_load_dwordx4 %0, %4, %5" KVNT "\n\t"
          "global_load_dwordx4 %1, %4, %5 offset:1024" KVNT "\n\t"
          "global_load_dwordx4 %2, %4, %5 offset:2048" KVNT "\n\t"
          "global_load_dwordx4 %3, %4, %5 offset:3072" KVNT
          : "=v"(kv[u][o]), "=v"(kv[u][o + 1]), "=v"(kv[u][o + 2]), "=v"(kv[u][o + 3])
          : "v"(kvo), "s"(g) : "memory");
    };
#define KV_PIN(u, o, N) asm volatile("s_waitcnt vmcnt(%8)" : "+v"(kv[u][o]), "+v"(kv[u][o + 1]), "+v"(kv[u][o + 2]), "+v"(kv[u][o + 3]), \
                                     "+v"(kv[u][o + 4]), "+v"(kv[u][o + 5]), "+v"(kv[u][o + 6]), "+v"(kv[u][o + 7]) : "i"(N) : "memory")
    auto putK = [&](int u) {
      char* kl = smem + 98304 + (c * 2 + u) * 8192 + (lane >> 4) * 256;
#pragma unroll
      for (int i = 0; i < 8; ++i) *(u32x4*)(kl + i * 1024 + (((lane & 15) ^ ((4 * i + (lane >> 4)) & 15)) << 4)) = kv[u][i];
    };
    auto putV = [&](int u) {
      char* vl = smem + u * 32768 + c * 8192 + (lane >> 4) * 256 + (((lane & 15) ^ ((lane >> 4) << 2)) << 4);
#pragma unroll
      for (int i = 0; i < 8; ++i) *(u32x4*)(vl + i * 1024) = kv[u][8 + i];
    };
    stage(0, 0);
    stage(1, K1_STAGE);
    stage(2, 2 * K1_STAGE);
    kvchunk(0);
    kvchunk(1);
    asm volatile("s_waitcnt vmcnt(32)" ::: "memory");
    __builtin_amdgcn_s_barrier();
    MEMFENCE();
    unsigned pf0 = 0, pf1 = 0;
#pragma unroll
    for (int st = 0; st < 8; ++st) {
      if (st == 1) asm volatile("s_waitcnt vmcnt(24)" ::: "memory");
      else if (st < 6) asm volatile("s_waitcnt vmcnt(20)" ::: "memory");
      else if (st == 6) asm volatile("s_waitcnt vmcnt(8)" ::: "memory");
      __builtin_amdgcn_s_barrier();
      MEMFENCE();
      if (st < 5) stage(st + 3, (st % 3) * K1_STAGE);
      if (st < 6) kvchunk(st + 2);
      if (st == 5) {
        KV_PIN(0, 0, 24); KV_PIN(1, 0, 24);
        putK(0); putK(1);
      }
      if (st == 6) {
        const char* pw = (const char*)(X ? woB : woA) + (size_t)((xcd & 3) * 32 + idx) * 32768 + c * 8192;
        asm volatile("global_load_dword %0, %2, %3\n\tglobal_load_dword %1, %2, %3 offset:64"
                     : "=v"(pf0), "=v"(pf1) : "v"(lane * 128), "s"(pw) : "memory");
        KV_PIN(0, 8, 10);
        putV(0);
      }
    }
    KV_PIN(1, 8, 2);
    putV(1);
    pfk0 = pf0; pfk1 = pf1;
    asm volatile("s_waitcnt lgkmcnt(0)" ::: "memory");
    __builtin_amdgcn_s_barrier();
    MEMFENCE();
    const u32x4* qs = (const u32x4*)(smem + 65536 + c * 8192) + lane;
#pragma unroll
    for (int i = 0; i < 8; ++i) qf[i] = __builtin_bit_cast(bf16x8, qs[i * 64]);
  }

  const int unit = c * 2 + (w >> 2), b = bg * 8 + unit;
  const char* kl = smem + 98304 + unit * 8192;
  const char* vl = smem + (w >> 2) * 32768 + c * 8192;
  const int pr = (r & 0x13) | ((r & 4) << 1) | ((r & 8) >> 1);
  f32x16 sacc, sac1;
#pragma unroll
  for (int i = 0; i < 16; ++i) { sacc[i] = 0.f; sac1[i] = 0.f; }
#pragma unroll
  for (int i = 0; i < 8; i += 2) {
    sacc = MFMA32(*(const bf16x8*)(kl + pr * 256 + (((2 * i + hh) ^ (pr & 15)) << 4)), qf[i], sacc);
    sac1 = MFMA32(*(const bf16x8*)(kl + pr * 256 + (((2 * i + 2 + hh) ^ (pr & 15)) << 4)), qf[i + 1], sac1);
  }
  sacc += sac1;

  const float scale = 0.08838834764831845f;
  float m = -3.0e38f;
#pragma unroll
  for (int i = 0; i < 16; i += 2) {
    unsigned u = cvtpk(sacc[i], sacc[i + 1]);
    float a = __uint_as_float(u << 16) * scale, cc = __uint_as_float(u & 0xffff0000u) * scale;
    unsigned u2 = cvtpk(a, cc);
    sacc[i] = __uint_as_float(u2 << 16);
    sacc[i + 1] = __uint_as_float(u2 & 0xffff0000u);
    m = fmaxf(m, fmaxf(sacc[i], sacc[i + 1]));
  }
  {
    auto rr = __builtin_amdgcn_permlane32_swap(__float_as_uint(m), __float_as_uint(m), false, false);
    m = fmaxf(__uint_as_float(rr[0]), __uint_as_float(rr[1]));
  }
  float sum = 0.f;
#pragma unroll
  for (int i = 0; i < 16; ++i) {
    sacc[i] = __builtin_amdgcn_exp2f((sacc[i] - m) * 1.4426950408889634f);
    sum += sacc[i];
  }
  {
    auto rr = __builtin_amdgcn_permlane32_swap(__float_as_uint(sum), __float_as_uint(sum), false, false);
    sum = __uint_as_float(rr[0]) + __uint_as_float(rr[1]);
  }
  const float inv = 1.0f / sum;
#pragma unroll
  for (int i = 0; i < 16; ++i) sacc[i] *= inv;
  bf16x8 pf0 = pack8(sacc, 0), pf1 = pack8(sacc, 1);

  u32x4* od = (u32x4*)(ows + ((size_t)((X * 32 + b) * 256 + h * 8) * 64 + lane) * 8);
#pragma unroll
  for (int t = 0; t < 4; ++t) {
    f32x16 o;
#pragma unroll
    for (int i = 0; i < 16; ++i) o[i] = 0.f;
    const char* pa = vl + tro[t];
    o = MFMA32(tr_frag(pa, pa + 1024), pf0, o);
    o = MFMA32(tr_frag(pa + 4096, pa + 4096 + 1024), pf1, o);
    {
      u32x4 v0 = __builtin_bit_cast(u32x4, pack8(o, 0)), v1 = __builtin_bit_cast(u32x4, pack8(o, 1));
      asm volatile("global_store_dwordx4 %0, %1, off sc1\n\tglobal_store_dwordx4 %0, %2, off offset:1024 sc1\n\ts_nop 1"
                   ::"v"(od + (2 * t) * 64), "v"(v0), "v"(v1) : "memory");
    }
  }
  asm volatile("s_waitcnt vmcnt(0)" : "+v"(pfk0), "+v"(pfk1)::"memory");
}

constexpr int K2_NB = 8;
constexpr int K2_LDS = K2_NB * 16384;
constexpr int K2_OD = 16;

__global__ __launch_bounds__(512, 2) void k2_oproj(const uint16_t* __restrict__ woA, const uint16_t* __restrict__ woB,
                                                   const uint16_t* __restrict__ ows, float* __restrict__ slab,
                                                   unsigned* __restrict__ cnt, uint16_t* __restrict__ out) {
  extern __shared__ __attribute__((aligned(16))) char smem[];
  const int tid = threadIdx.x, lane = tid & 63;
  const int w = __builtin_amdgcn_readfirstlane(tid >> 6);
  const int bid = blockIdx.x, xcd = bid & 7, idx = bid >> 3;
  const int X = xcd >> 2, hg = xcd & 3, rt = idx >> 2, ct = idx & 3;
  const int c = w & 3;
  const int tile = (X * 8 + rt) * 4 + ct;

  if (w < 4) {
    const int hh = lane >> 5, blk = (lane >> 4) & 1, q = (lane & 15) >> 2, p = lane & 3;
    const int troff = (8 * hh + q) * 256 + 32 * blk + 16 * (p & 1) + 8 * (p >> 1);
    unsigned pa[2][4];
#pragma unroll
    for (int t = 0; t < 4; ++t) {
      pa[0][t] = lds_addr(smem) + troff + ((t ^ q) << 6);
      pa[1][t] = pa[0][t] + 65536;
      asm volatile("" : "+v"(pa[0][t]), "+v"(pa[1][t]));
    }
    const char* ob = (const char*)ows + (size_t)((X * 32 + rt * 4 + c) * 256 + hg * 64) * 1024;
    const unsigned oo = lane * 16;
    u32x4 of[K2_OD];
    f32x16 acc[4];
#pragma unroll
    for (int t = 0; t < 4; ++t)
#pragma unroll
      for (int i = 0; i < 16; ++i) acc[t][i] = 0.f;
    bf16x8 af[2][4];
    auto oload = [&](int ks) {
      asm volatile("global_load_dwordx4 %0, %1, %2" : "=v"(of[ks % K2_OD]) : "v"(oo), "s"(ob + ks * 1024) : "memory");
    };
    auto rdA = [&](int ks, int t) {
      const int o = ((ks >> 2) % K2_NB) * 16384 + (ks & 3) * 4096;
      const unsigned a = pa[o >> 16][t] + (o & 65535);
      s16x4 lo = __builtin_amdgcn_ds_read_tr16_b64_v4i16((lds_s16x4_ptr)(size_t)a);
      s16x4 hi = __builtin_amdgcn_ds_read_tr16_b64_v4i16((lds_s16x4_ptr)(size_t)(a + 1024));
      af[ks & 1][t] = __builtin_bit_cast(bf16x8, __builtin_shufflevector(lo, hi, 0, 1, 2, 3, 4, 5, 6, 7));
    };
#pragma unroll
    for (int ks = 0; ks < K2_OD; ++ks) oload(ks);
    __builtin_amdgcn_s_barrier();
    MEMFENCE();
#pragma unroll
    for (int t = 0; t < 4; ++t) rdA(0, t);
    SCHED0();
#pragma unroll
    for (int ks = 0; ks < 64; ++ks) {
      if ((ks & 3) == 3 && ks < 63) {
        asm volatile("s_waitcnt lgkmcnt(0)" ::: "memory");
        __builtin_amdgcn_s_barrier();
        MEMFENCE();
      }
      {
        constexpr int dummy = 0; (void)dummy;
        const int n = (63 - ks < K2_OD - 1) ? 63 - ks : K2_OD - 1;
        asm volatile("s_waitcnt vmcnt(%1)" : "+v"(of[ks % K2_OD]) : "i"(n) : "memory");
      }
      const bf16x8 bfr = __builtin_bit_cast(bf16x8, of[ks % K2_OD]);
      acc[0] = MFMA32(af[ks & 1][0], bfr, acc[0]); SCHED0();
      if (ks < 63) { rdA(ks + 1, 0); rdA(ks + 1, 1); } SCHED0();
      acc[1] = MFMA32(af[ks & 1][1], bfr, acc[1]); SCHED0();
      if (ks < 63) { rdA(ks + 1, 2); rdA(ks + 1, 3); } SCHED0();
      acc[2] = MFMA32(af[ks & 1][2], bfr, acc[2]);
      acc[3] = MFMA32(af[ks & 1][3], bfr, acc[3]); SCHED0();
      if (ks + K2_OD < 64) oload(ks + K2_OD);
      SCHED0();
    }
    if (c == hg) {
      f32x4* sd = (f32x4*)smem + lane;
#pragma unroll
      for (int t = 0; t < 4; ++t)
#pragma unroll
        for (int g = 0; g < 4; ++g) sd[(t * 4 + g) * 64] = f32x4{acc[t][4 * g], acc[t][4 * g + 1], acc[t][4 * g + 2], acc[t][4 * g + 3]};
      asm volatile("s_waitcnt lgkmcnt(0)" ::: "memory");
    } else {
      const float* dst = slab + ((size_t)(tile * 4 + c) * 3 + (hg < c ? hg : hg - 1)) * SLAB_STRIDE;
      const unsigned so = lane * 16;
      asm volatile("s_nop 7\n\ts_nop 7" ::: "memory");
#pragma unroll
      for (int t = 0; t < 4; ++t) {
        f32x4 v0 = {acc[t][0], acc[t][1], acc[t][2], acc[t][3]}, v1 = {acc[t][4], acc[t][5], acc[t][6], acc[t][7]};
        f32x4 v2 = {acc[t][8], acc[t][9], acc[t][10], acc[t][11]}, v3 = {acc[t][12], acc[t][13], acc[t][14], acc[t][15]};
        asm volatile("global_store_dwordx4 %0, %2, %1 sc1\n\t"
                     "global_store_dwordx4 %0, %3, %1 offset:1024 sc1\n\t"
                     "global_store_dwordx4 %0, %4, %1 offset:2048 sc1\n\t"
                     "global_store_dwordx4 %0, %5, %1 offset:3072 sc1\n\ts_nop 1"
                     ::"v"(so), "s"(dst + t * 1024), "v"(v0), "v"(v1), "v"(v2), "v"(v3) : "memory");
      }
      asm volatile("s_waitcnt vmcnt(0)" ::: "memory");
      if (lane == 0) __hip_atomic_fetch_add(cnt + (tile * 4 + c) * 16, 1u, __ATOMIC_RELAXED, __HIP_MEMORY_SCOPE_AGENT);
    }
  } else {
    const uint16_t* wo = X ? woB : woA;
    const char* wb = (const char*)(wo + (size_t)(hg * 1024 + c * 16) * 512 + ct * 128);
    const unsigned woff = (lane >> 4) * 1024 + (((lane & 15) ^ ((lane >> 4) << 2)) << 4);
    const unsigned sbase = lds_addr(smem) + c * 4096;
    auto stage = [&](int st) {
#pragma unroll
      for (int j = 0; j < 4; ++j) glds16<0>(wb + (size_t)st * 65536 + j * 4096, woff, sbase + (st % K2_NB) * 16384 + j * 1024);
    };
#pragma unroll
    for (int st = 0; st < K2_NB; ++st) stage(st);
    asm volatile("s_waitcnt vmcnt(%0)" ::"i"(4 * (K2_NB - 1)) : "memory");
    __builtin_amdgcn_s_barrier();
    MEMFENCE();
#pragma unroll
    for (int st = 0; st < 15; ++st) {
      constexpr int dummy = 0; (void)dummy;
      const int last = (st + K2_NB - 1 < 15) ? st + K2_NB - 1 : 15;
      asm volatile("s_waitcnt vmcnt(%0)" ::"i"(4 * (last - (st + 1))) : "memory");
      __builtin_amdgcn_s_barrier();
      MEMFENCE();
      if (st + K2_NB < 16) stage(st + K2_NB);
    }
    if (w == 4) {
      const unsigned* cp = cnt + (tile * 4 + hg) * 16;
      for (int spin = 0; spin < (1 << 20); ++spin) {
        if (__hip_atomic_load(cp, __ATOMIC_RELAXED, __HIP_MEMORY_SCOPE_AGENT) >= 3u) break;
        __builtin_amdgcn_s_sleep(2);
      }
    }
  }
  MEMFENCE();
  __builtin_amdgcn_s_barrier();
  MEMFENCE();
  {
    const float* src = slab + (size_t)(tile * 4 + hg) * 3 * SLAB_STRIDE + w * 512;
    const unsigned so = lane * 16;
    f32x4 a0, a1, b0, b1, c0, c1;
    asm volatile("global_load_dwordx4 %0, %6, %7 sc1\n\t"
                 "global_load_dwordx4 %1, %6, %7 offset:1024 sc1\n\t"
                 "global_load_dwordx4 %2, %6, %8 sc1\n\t"
                 "global_load_dwordx4 %3, %6, %8 offset:1024 sc1\n\t"
                 "global_load_dwordx4 %4, %6, %9 sc1\n\t"
                 "global_load_dwordx4 %5, %6, %9 offset:1024 sc1\n\t"
                 "s_waitcnt vmcnt(0)"
                 : "=&v"(a0), "=&v"(a1), "=&v"(b0), "=&v"(b1), "=&v"(c0), "=&v"(c1)
                 : "v"(so), "s"(src), "s"(src + SLAB_STRIDE), "s"(src + 2 * SLAB_STRIDE) : "memory");
    const f32x4* own = (const f32x4*)smem + (w * 2) * 64 + lane;
    const f32x4 o0 = own[0], o1 = own[64];
    f32x4 s0, s1;
    if (hg == 0) { s0 = ((o0 + a0) + b0) + c0; s1 = ((o1 + a1) + b1) + c1; }
    else if (hg == 1) { s0 = ((a0 + o0) + b0) + c0; s1 = ((a1 + o1) + b1) + c1; }
    else if (hg == 2) { s0 = ((a0 + b0) + o0) + c0; s1 = ((a1 + b1) + o1) + c1; }
    else { s0 = ((a0 + b0) + c0) + o0; s1 = ((a1 + b1) + c1) + o1; }
    u32x4 o = {cvtpk(s0[0], s0[1]), cvtpk(s0[2], s0[3]), cvtpk(s1[0], s1[1]), cvtpk(s1[2], s1[3])};
    uint16_t* dst = out + (size_t)((rt * 4 + hg) * 32 + (lane & 31)) * 1024 + X * 512 + ct * 128 + (w >> 1) * 32 + (w & 1) * 16 + (lane >> 5) * 8;
    *(u32x4*)dst = o;
  }
}

extern "C" void kernel_launch(void* const* d_in, const int* in_sizes, int n_in, void* d_out, int out_size,
                              void* d_ws, size_t ws_size, hipStream_t stream) {
  (void)in_sizes; (void)n_in; (void)out_size; (void)ws_size;
  (void)hipFuncSetAttribute((const void*)k1_qproj_attn, hipFuncAttributeMaxDynamicSharedMemorySize, K1_LDS);
  (void)hipFuncSetAttribute((const void*)k2_oproj, hipFuncAttributeMaxDynamicSharedMemorySize, K2_LDS);
  const uint16_t* xA = (const uint16_t*)d_in[0];
  const uint16_t* kA = (const uint16_t*)d_in[1];
  const uint16_t* vA = (const uint16_t*)d_in[2];
  const uint16_t* wqA = (const uint16_t*)d_in[3];
  const uint16_t* woA = (const uint16_t*)d_in[4];
  const uint16_t* xB = (const uint16_t*)d_in[5];
  const uint16_t* kB = (const uint16_t*)d_in[6];
  const uint16_t* vB = (const uint16_t*)d_in[7];
  const uint16_t* wqB = (const uint16_t*)d_in[8];
  const uint16_t* woB = (const uint16_t*)d_in[9];
  uint16_t* ows = (uint16_t*)d_ws;
  float* slab = (float*)((char*)d_ws + (16u << 20));
  unsigned* cnt = (unsigned*)((char*)d_ws + (32u << 20));
  k1_qproj_attn<<<256, 512, K1_LDS, stream>>>(xA, kA, vA, wqA, xB, kB, vB, wqB, ows, woA, woB, cnt, slab, (const uint16_t*)d_out);
  k2_oproj<<<256, 512, K2_LDS, stream>>>(woA, woB, ows, slab, cnt, (uint16_t*)d_out);
}
```
